```python
import torch

def _softmax(x, axis=-1):
    return torch.softmax(x, dim=axis)
def _gelu(x):
    return torch.nn.functional.gelu(x, approximate="tanh")
def _silu(x):
    return torch.nn.functional.silu(x)
def _sigmoid(x):
    return torch.sigmoid(x)
def _layernorm(x, axis=-1, eps=1e-5):
    m = x.mean(dim=axis, keepdim=True)
    v = x.var(dim=axis, keepdim=True, unbiased=False)
    return (x - m) / torch.sqrt(v + eps)
def _scatter_add(tgt, idx, src, axis=0):
    return tgt.clone().index_add_(axis, idx, src)
def _segment_sum(src, seg, n):
    out = torch.zeros((n,) + src.shape[1:], dtype=src.dtype)
    return out.index_add_(0, seg, src)
def _pad(x, pw):
    flat = tuple(v for pair in reversed(pw) for v in pair)
    return torch.nn.functional.pad(x, flat)

kernel_name = 'standalone__mha_softmax__sampling'

def setup_inputs():

    _w = (lambda a: (lambda t: t.to(torch.bfloat16) if t.is_floating_point() else t)(torch.as_tensor(a)))
    B, H, Sq, Sk, D = 8, 32, 1, 8192, 128
    return {
        "q": _w((lambda *s: torch.randn(*s, dtype=torch.bfloat16) * 0.8)(B, H, Sq, D)),
        "k": _w((lambda *s: torch.randn(*s, dtype=torch.bfloat16) * 0.8)(B, H, Sk, D)),
        "v": _w((lambda *s: torch.randn(*s, dtype=torch.bfloat16) * 0.05)(B, H, Sk, D)),
    }

def reference(q, k, v):
    scale = q.shape[-1] ** -0.5
    scores = torch.einsum("bhqd,bhkd->bhqk", q, k) * scale
    p = _softmax(scores, axis=-1)
    return torch.einsum("bhqk,bhkd->bhqd", p, v)
```

```cpp
#include <hip/hip_runtime.h>
#include <hip/hip_bf16.h>
#include <stdint.h>

#define NW 16
#define DEPTH 2
#define AUX 2

constexpr int SK = 8192, D = 128;
constexpr int TT = 4;
constexpr int NTILE = SK / (4 * TT * NW);

using u32x4 = __attribute__((ext_vector_type(4))) unsigned;
using f32x4 = __attribute__((ext_vector_type(4))) float;
using bf16x2 = __attribute__((ext_vector_type(2))) __bf16;

__device__ __forceinline__ float dot2(unsigned a, unsigned b, float acc) {
  return __builtin_amdgcn_fdot2_f32_bf16(__builtin_bit_cast(bf16x2, a), __builtin_bit_cast(bf16x2, b), acc, false);
}
template <int CTRL> __device__ __forceinline__ float dpp(float x) {
  return __builtin_bit_cast(float, __builtin_amdgcn_mov_dpp(__builtin_bit_cast(int, x), CTRL, 0xf, 0xf, true));
}
constexpr int XOR1 = 0xB1, XOR2 = 0x4E, XOR7 = 0x141, XOR8 = 0x128;
__device__ __forceinline__ float row16_sum(float x) {
  x += dpp<XOR1>(x); x += dpp<XOR2>(x); x += dpp<XOR7>(x); x += dpp<XOR8>(x); return x;
}
__device__ __forceinline__ float xrow_max(float x) {
  auto s = __builtin_amdgcn_permlane16_swap(__float_as_uint(x), __float_as_uint(x), false, false);
  x = fmaxf(__uint_as_float(s[0]), __uint_as_float(s[1]));
  auto t = __builtin_amdgcn_permlane32_swap(__float_as_uint(x), __float_as_uint(x), false, false);
  return fmaxf(__uint_as_float(t[0]), __uint_as_float(t[1]));
}
__device__ __forceinline__ float xrow_sum(float x) {
  auto s = __builtin_amdgcn_permlane16_swap(__float_as_uint(x), __float_as_uint(x), false, false);
  x = __uint_as_float(s[0]) + __uint_as_float(s[1]);
  auto t = __builtin_amdgcn_permlane32_swap(__float_as_uint(x), __float_as_uint(x), false, false);
  return __uint_as_float(t[0]) + __uint_as_float(t[1]);
}

__global__ void __launch_bounds__(NW * 64) attn_decode(const uint16_t* __restrict__ q, const uint16_t* __restrict__ k,
                                                       const uint16_t* __restrict__ v, uint16_t* __restrict__ out) {
  __shared__ __attribute__((aligned(16))) float lds[NW * (D + 4)];
  const int bh = blockIdx.x;
  const int tid = threadIdx.x, lane = tid & 63, wave = __builtin_amdgcn_readfirstlane(tid >> 6);
  const int c = lane & 15, r = lane >> 4;
  constexpr float C2 = 0.088388347648318440f * 1.4426950408889634f;
  constexpr unsigned HEAD_BYTES = SK * D * 2;
  const auto krs = __builtin_amdgcn_make_buffer_rsrc((void*)(k + (size_t)bh * SK * D), 0, HEAD_BYTES, 0x00020000);
  const auto vrs = __builtin_amdgcn_make_buffer_rsrc((void*)(v + (size_t)bh * SK * D), 0, HEAD_BYTES, 0x00020000);
  const unsigned voff = lane * 16;
  const u32x4 qp = *reinterpret_cast<const u32x4*>(q + bh * D + 8 * c);

  u32x4 Kb[DEPTH][TT], Vb[DEPTH][TT];
  auto issue = [&](int slot, int it) {
    const unsigned so = (unsigned)(it * NW + wave) * (TT * 1024);
#pragma unroll
    for (int j = 0; j < TT; ++j) Kb[slot][j] = __builtin_amdgcn_raw_buffer_load_b128(krs, voff + j * 1024, so, AUX);
#pragma unroll
    for (int j = 0; j < TT; ++j) Vb[slot][j] = __builtin_amdgcn_raw_buffer_load_b128(vrs, voff + j * 1024, so, AUX);
  };
  float m = -INFINITY, l = 0.f, acc[8];
#pragma unroll
  for (int i = 0; i < 8; ++i) acc[i] = 0.f;
  auto compute = [&](const u32x4 (&Kc)[TT], const u32x4 (&Vc)[TT]) {
    float s[TT];
#pragma unroll
    for (int j = 0; j < TT; ++j) {
      float d = dot2(Kc[j][0], qp[0], 0.f);
      d = dot2(Kc[j][1], qp[1], d); d = dot2(Kc[j][2], qp[2], d); d = dot2(Kc[j][3], qp[3], d);
      s[j] = row16_sum(d);
    }
    float mn = m;
#pragma unroll
    for (int j = 0; j < TT; ++j) mn = fmaxf(mn, s[j]);
    const float mc = mn * C2;
    const float alpha = __builtin_amdgcn_exp2f(fmaf(m, C2, -mc));
    m = mn;
    float p[TT], ps = 0.f;
#pragma unroll
    for (int j = 0; j < TT; ++j) { p[j] = __builtin_amdgcn_exp2f(fmaf(s[j], C2, -mc)); ps += p[j]; }
    l = fmaf(l, alpha, ps);
#pragma unroll
    for (int i = 0; i < 8; ++i) acc[i] *= alpha;
#pragma unroll
    for (int j = 0; j < TT; ++j)
#pragma unroll
      for (int i = 0; i < 4; ++i) {
        acc[2 * i] = fmaf(p[j], __builtin_bit_cast(float, Vc[j][i] << 16), acc[2 * i]);
        acc[2 * i + 1] = fmaf(p[j], __builtin_bit_cast(float, Vc[j][i] & 0xffff0000u), acc[2 * i + 1]);
      }
  };
  static_assert(NTILE % DEPTH == 0, "");
#pragma unroll
  for (int d = 0; d < DEPTH; ++d) issue(d, d);
  for (int it = 0; it < NTILE - DEPTH; it += DEPTH) {
#pragma unroll
    for (int d = 0; d < DEPTH; ++d) {
      u32x4 Kc[TT], Vc[TT];
#pragma unroll
      for (int j = 0; j < TT; ++j) { Kc[j] = Kb[d][j]; Vc[j] = Vb[d][j]; }
      compute(Kc, Vc);
      __builtin_amdgcn_sched_barrier(0);
      issue(d, it + DEPTH + d);
      __builtin_amdgcn_sched_barrier(0);
    }
  }
#pragma unroll
  for (int d = 0; d < DEPTH; ++d) compute(Kb[d], Vb[d]);

  const float M = xrow_max(m);
  const float w = __builtin_amdgcn_exp2f((m - M) * C2);
  l = xrow_sum(l * w);
#pragma unroll
  for (int i = 0; i < 8; ++i) acc[i] = xrow_sum(acc[i] * w);
  float* rec = lds + wave * (D + 4);
  if (r == 0) {
    *reinterpret_cast<f32x4*>(rec + 4 + 8 * c) = f32x4{acc[0], acc[1], acc[2], acc[3]};
    *reinterpret_cast<f32x4*>(rec + 8 + 8 * c) = f32x4{acc[4], acc[5], acc[6], acc[7]};
    if (c == 0) { rec[0] = M; rec[1] = l; }
  }
  __syncthreads();
  if (tid < 64) {
    float MM = -INFINITY;
#pragma unroll
    for (int x = 0; x < NW; ++x) MM = fmaxf(MM, lds[x * (D + 4)]);
    float L = 0.f, o0 = 0.f, o1 = 0.f;
#pragma unroll
    for (int x = 0; x < NW; ++x) {
      const float* rx = lds + x * (D + 4);
      const float wt = __builtin_amdgcn_exp2f((rx[0] - MM) * C2);
      L = fmaf(wt, rx[1], L);
      o0 = fmaf(wt, rx[4 + 2 * tid], o0);
      o1 = fmaf(wt, rx[5 + 2 * tid], o1);
    }
    const float inv = 1.f / L;
    __hip_bfloat162 o;
    o.x = __float2bfloat16(o0 * inv); o.y = __float2bfloat16(o1 * inv);
    reinterpret_cast<__hip_bfloat162*>(out + bh * D)[tid] = o;
  }
}

extern "C" void kernel_launch(void* const* d_in, const int* in_sizes, int n_in, void* d_out, int out_size, void* d_ws,
                              size_t ws_size, hipStream_t stream) {
  const int BH = in_sizes[0] / D;
  attn_decode<<<BH, NW * 64, 0, stream>>>((const uint16_t*)d_in[0], (const uint16_t*)d_in[1], (const uint16_t*)d_in[2],
                                          (uint16_t*)d_out);
}
```

```cpp
#include <hip/hip_runtime.h>
#include <hip/hip_bf16.h>
#include <stdint.h>
typedef unsigned long long u64;
using u32x4 = __attribute__((ext_vector_type(4))) unsigned;
using f32x4 = __attribute__((ext_vector_type(4))) float;
using f32x2 = __attribute__((ext_vector_type(2))) float;
using bf16x2 = __attribute__((ext_vector_type(2))) __bf16;
constexpr int SK = 8192, D = 128;
constexpr unsigned HEADB = SK * D * 2;

__device__ __forceinline__ float dot2(unsigned a, unsigned b, float acc) {
  return __builtin_amdgcn_fdot2_f32_bf16(__builtin_bit_cast(bf16x2, a), __builtin_bit_cast(bf16x2, b), acc, false);
}
template <int CTRL> __device__ __forceinline__ float dpp(float x) {
  return __builtin_bit_cast(float, __builtin_amdgcn_mov_dpp(__builtin_bit_cast(int, x), CTRL, 0xf, 0xf, true));
}
constexpr int XOR1 = 0xB1, XOR2 = 0x4E, XOR7 = 0x141, XOR8 = 0x128;
__device__ __forceinline__ float row16_sum(float x) {
  x += dpp<XOR1>(x); x += dpp<XOR2>(x); x += dpp<XOR7>(x); x += dpp<XOR8>(x); return x;
}
__device__ __forceinline__ float xrow_max(float x) {
  auto s = __builtin_amdgcn_permlane16_swap(__float_as_uint(x), __float_as_uint(x), false, false);
  x = fmaxf(__uint_as_float(s[0]), __uint_as_float(s[1]));
  auto t = __builtin_amdgcn_permlane32_swap(__float_as_uint(x), __float_as_uint(x), false, false);
  return fmaxf(__uint_as_float(t[0]), __uint_as_float(t[1]));
}
__device__ __forceinline__ float xrow_sum(float x) {
  auto s = __builtin_amdgcn_permlane16_swap(__float_as_uint(x), __float_as_uint(x), false, false);
  x = __uint_as_float(s[0]) + __uint_as_float(s[1]);
  auto t = __builtin_amdgcn_permlane32_swap(__float_as_uint(x), __float_as_uint(x), false, false);
  return __uint_as_float(t[0]) + __uint_as_float(t[1]);
}
#define SB() __builtin_amdgcn_sched_barrier(0)

template <int NW, int TT, int R, int NU, int AUX>
__global__ void __launch_bounds__(NW * 64) attn_ring(const uint16_t* __restrict__ q, const uint16_t* __restrict__ k,
                                                     const uint16_t* __restrict__ v, uint16_t* __restrict__ out, u64* stamps) {
  u64 t0 = 0;
  if (stamps) t0 = __builtin_amdgcn_s_memrealtime();
  __shared__ __attribute__((aligned(16))) float lds[NW * (D + 4)];
  constexpr int P = 2 * TT, NS = NU * P, NTILE = SK / (4 * TT * NW);
  static_assert(R < NS && NTILE % NU == 0, "");
  const int bh = blockIdx.x;
  const int tid = threadIdx.x, lane = tid & 63, wave = __builtin_amdgcn_readfirstlane(tid >> 6);
  const int c = lane & 15, r = lane >> 4;
  constexpr float C2 = 0.088388347648318440f * 1.4426950408889634f;
  const auto krs = __builtin_amdgcn_make_buffer_rsrc((void*)(k + (size_t)bh * SK * D), 0, HEADB, 0x00020000);
  const auto vrs = __builtin_amdgcn_make_buffer_rsrc((void*)(v + (size_t)bh * SK * D), 0, HEADB, 0x00020000);
  const unsigned voff = lane * 16;
  u32x4 qp = *reinterpret_cast<const u32x4*>(q + bh * D + 8 * c);
  SB();
  u32x4 buf[NS];
  constexpr unsigned TSTRIDE = NW * TT * 1024;
  unsigned base = wave * TT * 1024;
#define LD(n) __builtin_amdgcn_raw_buffer_load_b128((((n) % P) < TT) ? krs : vrs, voff + (((n) % TT) & 3) * 1024, base + ((n) / P) * TSTRIDE + (((n) % TT) >> 2) * 4096, AUX)
#pragma unroll
  for (int n = 0; n < R; ++n) { buf[n] = LD(n); SB(); }
  float m = -INFINITY, l = 0.f;
  f32x2 acc[4];
#pragma unroll
  for (int i = 0; i < 4; ++i) acc[i] = f32x2{0.f, 0.f};
  float s[TT], p[TT];
  for (int it = 0; it < NTILE; it += NU) {
#pragma unroll
    for (int n = 0; n < NS; ++n) {
      asm volatile("" : "+v"(buf[n]) :: "memory");
      SB();
      buf[(n + R) % NS] = LD(n + R);
      SB();
      const int j = n % P;
      if (j < TT) {
        float d = dot2(buf[n][0], qp[0], 0.f);
        d = dot2(buf[n][1], qp[1], d); d = dot2(buf[n][2], qp[2], d); d = dot2(buf[n][3], qp[3], d);
        s[j] = row16_sum(d);
        if (j == TT - 1) {
          float mn = m;
#pragma unroll
          for (int x = 0; x < TT; ++x) mn = fmaxf(mn, s[x]);
          const float mc = mn * C2;
          const float alpha = __builtin_amdgcn_exp2f(fmaf(m, C2, -mc));
          m = mn;
          float ps = 0.f;
#pragma unroll
          for (int x = 0; x < TT; ++x) { p[x] = __builtin_amdgcn_exp2f(fmaf(s[x], C2, -mc)); ps += p[x]; }
          l = fmaf(l, alpha, ps);
#pragma unroll
          for (int i = 0; i < 4; ++i) acc[i] *= alpha;
          asm volatile("" : "+v"(acc[0]), "+v"(acc[1]), "+v"(acc[2]), "+v"(acc[3]), "+v"(l));
        } else {
          asm volatile("" : "+v"(s[j]));
        }
      } else {
        const float pj = p[j - TT];
#pragma unroll
        for (int i = 0; i < 4; ++i) {
          const f32x2 vv = {__builtin_bit_cast(float, buf[n][i] << 16), __builtin_bit_cast(float, buf[n][i] & 0xffff0000u)};
          acc[i] = __builtin_elementwise_fma(f32x2{pj, pj}, vv, acc[i]);
        }
        asm volatile("" : "+v"(acc[0]), "+v"(acc[1]), "+v"(acc[2]), "+v"(acc[3]));
      }
      SB();
    }
    base += NU * TSTRIDE;
  }
#undef LD
  const float M = xrow_max(m);
  const float w = __builtin_amdgcn_exp2f((m - M) * C2);
  l = xrow_sum(l * w);
  float a[8];
#pragma unroll
  for (int i = 0; i < 4; ++i) { a[2 * i] = xrow_sum(acc[i][0] * w); a[2 * i + 1] = xrow_sum(acc[i][1] * w); }
  float* rec = lds + wave * (D + 4);
  if (r == 0) {
    *reinterpret_cast<f32x4*>(rec + 4 + 8 * c) = f32x4{a[0], a[1], a[2], a[3]};
    *reinterpret_cast<f32x4*>(rec + 8 + 8 * c) = f32x4{a[4], a[5], a[6], a[7]};
    if (c == 0) { rec[0] = M; rec[1] = l; }
  }
  __syncthreads();
  if (tid < 64) {
    float MM = -INFINITY;
#pragma unroll
    for (int x = 0; x < NW; ++x) MM = fmaxf(MM, lds[x * (D + 4)]);
    float L = 0.f, o0 = 0.f, o1 = 0.f;
#pragma unroll
    for (int x = 0; x < NW; ++x) {
      const float* rx = lds + x * (D + 4);
      const float wt = __builtin_amdgcn_exp2f((rx[0] - MM) * C2);
      L = fmaf(wt, rx[1], L);
      o0 = fmaf(wt, rx[4 + 2 * tid], o0);
      o1 = fmaf(wt, rx[5 + 2 * tid], o1);
    }
    const float inv = 1.f / L;
    __hip_bfloat162 o;
    o.x = __float2bfloat16(o0 * inv); o.y = __float2bfloat16(o1 * inv);
    reinterpret_cast<__hip_bfloat162*>(out + bh * D)[tid] = o;
    if (stamps && tid == 0) {
      u64 t1 = __builtin_amdgcn_s_memrealtime();
      unsigned xcc; asm volatile("s_getreg_b32 %0, hwreg(HW_REG_XCC_ID)" : "=s"(xcc));
      unsigned hwid; asm volatile("s_getreg_b32 %0, hwreg(HW_REG_HW_ID)" : "=s"(hwid));
      stamps[blockIdx.x * 4] = t0; stamps[blockIdx.x * 4 + 1] = t1; stamps[blockIdx.x * 4 + 2] = xcc; stamps[blockIdx.x * 4 + 3] = hwid;
    }
  }
}

extern "C" void kernel_launch(void* const* d_in, const int* in_sizes, int n_in, void* d_out, int out_size, void* d_ws,
                              size_t ws_size, hipStream_t stream) {
  const int BH = in_sizes[0] / D;
  attn_ring<4,4,5,1,2><<<BH, 256, 0, stream>>>((const uint16_t*)d_in[0], (const uint16_t*)d_in[1], (const uint16_t*)d_in[2], (uint16_t*)d_out, nullptr);
}
```

```cpp
#include <hip/hip_runtime.h>
#include <hip/hip_bf16.h>
#include <stdint.h>
#include <stdio.h>
#include <vector>
#include <algorithm>
#include <unistd.h>
typedef unsigned long long u64;
using u32x4 = __attribute__((ext_vector_type(4))) unsigned;
using f32x4 = __attribute__((ext_vector_type(4))) float;
using f32x2 = __attribute__((ext_vector_type(2))) float;
using bf16x2 = __attribute__((ext_vector_type(2))) __bf16;
constexpr int SK = 8192, D = 128;
constexpr unsigned HEADB = SK * D * 2;

__device__ __forceinline__ float dot2(unsigned a, unsigned b, float acc) {
  return __builtin_amdgcn_fdot2_f32_bf16(__builtin_bit_cast(bf16x2, a), __builtin_bit_cast(bf16x2, b), acc, false);
}
template <int CTRL> __device__ __forceinline__ float dpp(float x) {
  return __builtin_bit_cast(float, __builtin_amdgcn_mov_dpp(__builtin_bit_cast(int, x), CTRL, 0xf, 0xf, true));
}
constexpr int XOR1 = 0xB1, XOR2 = 0x4E, XOR7 = 0x141, XOR8 = 0x128;
__device__ __forceinline__ float row16_sum(float x) {
  x += dpp<XOR1>(x); x += dpp<XOR2>(x); x += dpp<XOR7>(x); x += dpp<XOR8>(x); return x;
}
__device__ __forceinline__ float xrow_max(float x) {
  auto s = __builtin_amdgcn_permlane16_swap(__float_as_uint(x), __float_as_uint(x), false, false);
  x = fmaxf(__uint_as_float(s[0]), __uint_as_float(s[1]));
  auto t = __builtin_amdgcn_permlane32_swap(__float_as_uint(x), __float_as_uint(x), false, false);
  return fmaxf(__uint_as_float(t[0]), __uint_as_float(t[1]));
}
__device__ __forceinline__ float xrow_sum(float x) {
  auto s = __builtin_amdgcn_permlane16_swap(__float_as_uint(x), __float_as_uint(x), false, false);
  x = __uint_as_float(s[0]) + __uint_as_float(s[1]);
  auto t = __builtin_amdgcn_permlane32_swap(__float_as_uint(x), __float_as_uint(x), false, false);
  return __uint_as_float(t[0]) + __uint_as_float(t[1]);
}
#define SB() __builtin_amdgcn_sched_barrier(0)

template <int NW, int TT, int R, int NU, int AUX>
__global__ void __launch_bounds__(NW * 64) attn_ring(const uint16_t* __restrict__ q, const uint16_t* __restrict__ k,
                                                     const uint16_t* __restrict__ v, uint16_t* __restrict__ out, u64* stamps) {
  u64 t0 = 0;
  if (stamps) t0 = __builtin_amdgcn_s_memrealtime();
  __shared__ __attribute__((aligned(16))) float lds[NW * (D + 4)];
  constexpr int P = 2 * TT, NS = NU * P, NTILE = SK / (4 * TT * NW);
  static_assert(R < NS && NTILE % NU == 0, "");
  const int bh = blockIdx.x;
  const int tid = threadIdx.x, lane = tid & 63, wave = __builtin_amdgcn_readfirstlane(tid >> 6);
  const int c = lane & 15, r = lane >> 4;
  constexpr float C2 = 0.088388347648318440f * 1.4426950408889634f;
  const auto krs = __builtin_amdgcn_make_buffer_rsrc((void*)(k + (size_t)bh * SK * D), 0, HEADB, 0x00020000);
  const auto vrs = __builtin_amdgcn_make_buffer_rsrc((void*)(v + (size_t)bh * SK * D), 0, HEADB, 0x00020000);
  const unsigned voff = lane * 16;
  u32x4 qp = *reinterpret_cast<const u32x4*>(q + bh * D + 8 * c);
  SB();
  u32x4 buf[NS];
  constexpr unsigned TSTRIDE = NW * TT * 1024;
  unsigned base = wave * TT * 1024;
#define LD(n) __builtin_amdgcn_raw_buffer_load_b128((((n) % P) < TT) ? krs : vrs, voff + (((n) % TT) & 3) * 1024, base + ((n) / P) * TSTRIDE + (((n) % TT) >> 2) * 4096, AUX)
#pragma unroll
  for (int n = 0; n < R; ++n) { buf[n] = LD(n); SB(); }
  float m = -INFINITY, l = 0.f;
  f32x2 acc[4];
#pragma unroll
  for (int i = 0; i < 4; ++i) acc[i] = f32x2{0.f, 0.f};
  float s[TT], p[TT];
  for (int it = 0; it < NTILE; it += NU) {
#pragma unroll
    for (int n = 0; n < NS; ++n) {
      asm volatile("" : "+v"(buf[n]) :: "memory");
      SB();
      buf[(n + R) % NS] = LD(n + R);
      SB();
      const int j = n % P;
      if (j < TT) {
        float d = dot2(buf[n][0], qp[0], 0.f);
        d = dot2(buf[n][1], qp[1], d); d = dot2(buf[n][2], qp[2], d); d = dot2(buf[n][3], qp[3], d);
        s[j] = row16_sum(d);
        if (j == TT - 1) {
          float mn = m;
#pragma unroll
          for (int x = 0; x < TT; ++x) mn = fmaxf(mn, s[x]);
          const float mc = mn * C2;
          const float alpha = __builtin_amdgcn_exp2f(fmaf(m, C2, -mc));
          m = mn;
          float ps = 0.f;
#pragma unroll
          for (int x = 0; x < TT; ++x) { p[x] = __builtin_amdgcn_exp2f(fmaf(s[x], C2, -mc)); ps += p[x]; }
          l = fmaf(l, alpha, ps);
#pragma unroll
          for (int i = 0; i < 4; ++i) acc[i] *= alpha;
          asm volatile("" : "+v"(acc[0]), "+v"(acc[1]), "+v"(acc[2]), "+v"(acc[3]), "+v"(l));
        } else {
          asm volatile("" : "+v"(s[j]));
        }
      } else {
        const float pj = p[j - TT];
#pragma unroll
        for (int i = 0; i < 4; ++i) {
          const f32x2 vv = {__builtin_bit_cast(float, buf[n][i] << 16), __builtin_bit_cast(float, buf[n][i] & 0xffff0000u)};
          acc[i] = __builtin_elementwise_fma(f32x2{pj, pj}, vv, acc[i]);
        }
        asm volatile("" : "+v"(acc[0]), "+v"(acc[1]), "+v"(acc[2]), "+v"(acc[3]));
      }
      SB();
    }
    base += NU * TSTRIDE;
  }
#undef LD
  const float M = xrow_max(m);
  const float w = __builtin_amdgcn_exp2f((m - M) * C2);
  l = xrow_sum(l * w);
  float a[8];
#pragma unroll
  for (int i = 0; i < 4; ++i) { a[2 * i] = xrow_sum(acc[i][0] * w); a[2 * i + 1] = xrow_sum(acc[i][1] * w); }
  float* rec = lds + wave * (D + 4);
  if (r == 0) {
    *reinterpret_cast<f32x4*>(rec + 4 + 8 * c) = f32x4{a[0], a[1], a[2], a[3]};
    *reinterpret_cast<f32x4*>(rec + 8 + 8 * c) = f32x4{a[4], a[5], a[6], a[7]};
    if (c == 0) { rec[0] = M; rec[1] = l; }
  }
  __syncthreads();
  if (tid < 64) {
    float MM = -INFINITY;
#pragma unroll
    for (int x = 0; x < NW; ++x) MM = fmaxf(MM, lds[x * (D + 4)]);
    float L = 0.f, o0 = 0.f, o1 = 0.f;
#pragma unroll
    for (int x = 0; x < NW; ++x) {
      const float* rx = lds + x * (D + 4);
      const float wt = __builtin_amdgcn_exp2f((rx[0] - MM) * C2);
      L = fmaf(wt, rx[1], L);
      o0 = fmaf(wt, rx[4 + 2 * tid], o0);
      o1 = fmaf(wt, rx[5 + 2 * tid], o1);
    }
    const float inv = 1.f / L;
    __hip_bfloat162 o;
    o.x = __float2bfloat16(o0 * inv); o.y = __float2bfloat16(o1 * inv);
    reinterpret_cast<__hip_bfloat162*>(out + bh * D)[tid] = o;
    if (stamps && tid == 0) {
      u64 t1 = __builtin_amdgcn_s_memrealtime();
      unsigned xcc; asm volatile("s_getreg_b32 %0, hwreg(HW_REG_XCC_ID)" : "=s"(xcc));
      unsigned hwid; asm volatile("s_getreg_b32 %0, hwreg(HW_REG_HW_ID)" : "=s"(hwid));
      stamps[blockIdx.x * 4] = t0; stamps[blockIdx.x * 4 + 1] = t1; stamps[blockIdx.x * 4 + 2] = xcc; stamps[blockIdx.x * 4 + 3] = hwid;
    }
  }
}

__global__ void fillk(u32x4* p, size_t n, unsigned seed) {
  size_t i = blockIdx.x * (size_t)blockDim.x + threadIdx.x;
  for (; i < n; i += (size_t)gridDim.x * blockDim.x) {
    unsigned h = (unsigned)i * 2654435761u + seed; h ^= h >> 15; h *= 2246822519u; h ^= h >> 13;
    p[i] = u32x4{h, h * 3266489917u, h ^ 0x9e3779b9u, h * 668265263u};
  }
}
extern "C" void kernel_launch(void* const* d_in, const int* in_sizes, int n_in, void* d_out, int out_size, void* d_ws,
                              size_t ws_size, hipStream_t stream) {
  const int BH = in_sizes[0] / D;
  hipStreamCaptureStatus cs; hipStreamIsCapturing(stream, &cs);
  if (cs == hipStreamCaptureStatusNone) {
    hipDeviceProp_t pr; hipGetDeviceProperties(&pr, 0); fprintf(stderr, "DEV %s pci %d:%d clk %d mem %d\n", pr.name, pr.pciBusID, pr.pciDeviceID, pr.clockRate, pr.memoryClockRate);
    hipEvent_t e0, e1; hipEventCreate(&e0); hipEventCreate(&e1);
    u64* stamps = (u64*)((char*)d_ws + ws_size - (1 << 20));
    std::vector<u64> hs(256 * 4);
    for (int mode = 0; mode < 3; ++mode) {
      std::vector<float> cold, ink, warm;
      for (int rd = 0; rd < 9; ++rd) {
        fillk<<<2048, 256, 0, stream>>>((u32x4*)d_ws, (ws_size - (1 << 20)) / 16, rd);
        hipStreamSynchronize(stream);
        if (mode == 1) usleep(2000);
        if (mode == 2) usleep(30000);
        hipEventRecord(e0, stream);
        attn_ring<4,4,5,1,2><<<BH, 256, 0, stream>>>((const uint16_t*)d_in[0], (const uint16_t*)d_in[1], (const uint16_t*)d_in[2], (uint16_t*)d_out, stamps);
        hipEventRecord(e1, stream); hipEventSynchronize(e1);
        float ms; hipEventElapsedTime(&ms, e0, e1); cold.push_back(ms * 1e3f);
        hipMemcpy(hs.data(), stamps, 256 * 32, hipMemcpyDeviceToHost);
        u64 mn = ~0ull, mx = 0; for (int b = 0; b < 256; ++b) { mn = std::min(mn, hs[b * 4]); mx = std::max(mx, hs[b * 4 + 1]); }
        ink.push_back((mx - mn) * 0.01f);
        hipEventRecord(e0, stream);
        for (int r = 0; r < 10; ++r) attn_ring<4,4,5,1,2><<<BH, 256, 0, stream>>>((const uint16_t*)d_in[0], (const uint16_t*)d_in[1], (const uint16_t*)d_in[2], (uint16_t*)d_out, nullptr);
        hipEventRecord(e1, stream); hipEventSynchronize(e1);
        hipEventElapsedTime(&ms, e0, e1); warm.push_back(ms * 1e2f);
      }
      std::sort(cold.begin(), cold.end()); std::sort(ink.begin(), ink.end()); std::sort(warm.begin(), warm.end());
      fprintf(stderr, "MYBENCH mode %d cold ev %.2f (min %.2f) ink %.2f (min %.2f) warm %.2f (min %.2f)\n", mode, cold[4], cold[0], ink[4], ink[0], warm[4], warm[0]);
    }
  }
  attn_ring<4,4,5,1,2><<<BH, 256, 0, stream>>>((const uint16_t*)d_in[0], (const uint16_t*)d_in[1], (const uint16_t*)d_in[2], (uint16_t*)d_out, nullptr);
}
```

```cpp
#include <hip/hip_runtime.h>
#include <hip/hip_bf16.h>
#include <stdint.h>
typedef unsigned long long u64;
using u32x4 = __attribute__((ext_vector_type(4))) unsigned;
using f32x4 = __attribute__((ext_vector_type(4))) float;
using f32x2 = __attribute__((ext_vector_type(2))) float;
using bf16x2 = __attribute__((ext_vector_type(2))) __bf16;
constexpr int SK = 8192, D = 128;
constexpr unsigned HEADB = SK * D * 2;

__device__ __forceinline__ float dot2(unsigned a, unsigned b, float acc) {
  return __builtin_amdgcn_fdot2_f32_bf16(__builtin_bit_cast(bf16x2, a), __builtin_bit_cast(bf16x2, b), acc, false);
}
template <int CTRL> __device__ __forceinline__ float dpp(float x) {
  return __builtin_bit_cast(float, __builtin_amdgcn_mov_dpp(__builtin_bit_cast(int, x), CTRL, 0xf, 0xf, true));
}
constexpr int XOR1 = 0xB1, XOR2 = 0x4E, XOR7 = 0x141, XOR8 = 0x128;
__device__ __forceinline__ float row16_sum(float x) {
  x += dpp<XOR1>(x); x += dpp<XOR2>(x); x += dpp<XOR7>(x); x += dpp<XOR8>(x); return x;
}
__device__ __forceinline__ float xrow_max(float x) {
  auto s = __builtin_amdgcn_permlane16_swap(__float_as_uint(x), __float_as_uint(x), false, false);
  x = fmaxf(__uint_as_float(s[0]), __uint_as_float(s[1]));
  auto t = __builtin_amdgcn_permlane32_swap(__float_as_uint(x), __float_as_uint(x), false, false);
  return fmaxf(__uint_as_float(t[0]), __uint_as_float(t[1]));
}
__device__ __forceinline__ float xrow_sum(float x) {
  auto s = __builtin_amdgcn_permlane16_swap(__float_as_uint(x), __float_as_uint(x), false, false);
  x = __uint_as_float(s[0]) + __uint_as_float(s[1]);
  auto t = __builtin_amdgcn_permlane32_swap(__float_as_uint(x), __float_as_uint(x), false, false);
  return __uint_as_float(t[0]) + __uint_as_float(t[1]);
}
#define SB() __builtin_amdgcn_sched_barrier(0)

template <int NW, int TT, int R, int NU, int AUX>
__global__ void __launch_bounds__(NW * 64) attn_ring(const uint16_t* __restrict__ q, const uint16_t* __restrict__ k,
                                                     const uint16_t* __restrict__ v, uint16_t* __restrict__ out, u64* stamps) {
  u64 t0 = 0;
  if (stamps) t0 = __builtin_amdgcn_s_memrealtime();
  __shared__ __attribute__((aligned(16))) float lds[NW * (D + 4)];
  constexpr int P = 2 * TT, NS = NU * P, NTILE = SK / (4 * TT * NW);
  static_assert(R < NS && NTILE % NU == 0, "");
  const int bh = blockIdx.x;
  const int tid = threadIdx.x, lane = tid & 63, wave = __builtin_amdgcn_readfirstlane(tid >> 6);
  const int c = lane & 15, r = lane >> 4;
  constexpr float C2 = 0.088388347648318440f * 1.4426950408889634f;
  const auto krs = __builtin_amdgcn_make_buffer_rsrc((void*)(k + (size_t)bh * SK * D), 0, HEADB, 0x00020000);
  const auto vrs = __builtin_amdgcn_make_buffer_rsrc((void*)(v + (size_t)bh * SK * D), 0, HEADB, 0x00020000);
  const unsigned voff = lane * 16;
  u32x4 qp = *reinterpret_cast<const u32x4*>(q + bh * D + 8 * c);
  SB();
  u32x4 buf[NS];
  constexpr unsigned TSTRIDE = NW * TT * 1024;
  unsigned base = wave * TT * 1024;
#define LD(n) __builtin_amdgcn_raw_buffer_load_b128((((n) % P) < TT) ? krs : vrs, voff + (((n) % TT) & 3) * 1024, base + ((n) / P) * TSTRIDE + (((n) % TT) >> 2) * 4096, AUX)
#pragma unroll
  for (int n = 0; n < R; ++n) { buf[n] = LD(n); SB(); }
  float m = -INFINITY, l = 0.f;
  f32x2 acc[4];
#pragma unroll
  for (int i = 0; i < 4; ++i) acc[i] = f32x2{0.f, 0.f};
  float s[TT], p[TT];
  for (int it = 0; it < NTILE; it += NU) {
#pragma unroll
    for (int n = 0; n < NS; ++n) {
      asm volatile("" : "+v"(buf[n]) :: "memory");
      SB();
      buf[(n + R) % NS] = LD(n + R);
      SB();
      const int j = n % P;
      if (j < TT) {
        float d = dot2(buf[n][0], qp[0], 0.f);
        d = dot2(buf[n][1], qp[1], d); d = dot2(buf[n][2], qp[2], d); d = dot2(buf[n][3], qp[3], d);
        s[j] = row16_sum(d);
        if (j == TT - 1) {
          float mn = m;
#pragma unroll
          for (int x = 0; x < TT; ++x) mn = fmaxf(mn, s[x]);
          const float mc = mn * C2;
          const float alpha = __builtin_amdgcn_exp2f(fmaf(m, C2, -mc));
          m = mn;
          float ps = 0.f;
#pragma unroll
          for (int x = 0; x < TT; ++x) { p[x] = __builtin_amdgcn_exp2f(fmaf(s[x], C2, -mc)); ps += p[x]; }
          l = fmaf(l, alpha, ps);
#pragma unroll
          for (int i = 0; i < 4; ++i) acc[i] *= alpha;
          asm volatile("" : "+v"(acc[0]), "+v"(acc[1]), "+v"(acc[2]), "+v"(acc[3]), "+v"(l));
        } else {
          asm volatile("" : "+v"(s[j]));
        }
      } else {
        const float pj = p[j - TT];
#pragma unroll
        for (int i = 0; i < 4; ++i) {
          const f32x2 vv = {__builtin_bit_cast(float, buf[n][i] << 16), __builtin_bit_cast(float, buf[n][i] & 0xffff0000u)};
          acc[i] = __builtin_elementwise_fma(f32x2{pj, pj}, vv, acc[i]);
        }
        asm volatile("" : "+v"(acc[0]), "+v"(acc[1]), "+v"(acc[2]), "+v"(acc[3]));
      }
      SB();
    }
    base += NU * TSTRIDE;
  }
#undef LD
  const float M = xrow_max(m);
  const float w = __builtin_amdgcn_exp2f((m - M) * C2);
  l = xrow_sum(l * w);
  float a[8];
#pragma unroll
  for (int i = 0; i < 4; ++i) { a[2 * i] = xrow_sum(acc[i][0] * w); a[2 * i + 1] = xrow_sum(acc[i][1] * w); }
  float* rec = lds + wave * (D + 4);
  if (r == 0) {
    *reinterpret_cast<f32x4*>(rec + 4 + 8 * c) = f32x4{a[0], a[1], a[2], a[3]};
    *reinterpret_cast<f32x4*>(rec + 8 + 8 * c) = f32x4{a[4], a[5], a[6], a[7]};
    if (c == 0) { rec[0] = M; rec[1] = l; }
  }
  __syncthreads();
  if (tid < 64) {
    float MM = -INFINITY;
#pragma unroll
    for (int x = 0; x < NW; ++x) MM = fmaxf(MM, lds[x * (D + 4)]);
    float L = 0.f, o0 = 0.f, o1 = 0.f;
#pragma unroll
    for (int x = 0; x < NW; ++x) {
      const float* rx = lds + x * (D + 4);
      const float wt = __builtin_amdgcn_exp2f((rx[0] - MM) * C2);
      L = fmaf(wt, rx[1], L);
      o0 = fmaf(wt, rx[4 + 2 * tid], o0);
      o1 = fmaf(wt, rx[5 + 2 * tid], o1);
    }
    const float inv = 1.f / L;
    __hip_bfloat162 o;
    o.x = __float2bfloat16(o0 * inv); o.y = __float2bfloat16(o1 * inv);
    reinterpret_cast<__hip_bfloat162*>(out + bh * D)[tid] = o;
    if (stamps && tid == 0) {
      u64 t1 = __builtin_amdgcn_s_memrealtime();
      unsigned xcc; asm volatile("s_getreg_b32 %0, hwreg(HW_REG_XCC_ID)" : "=s"(xcc));
      unsigned hwid; asm volatile("s_getreg_b32 %0, hwreg(HW_REG_HW_ID)" : "=s"(hwid));
      stamps[blockIdx.x * 4] = t0; stamps[blockIdx.x * 4 + 1] = t1; stamps[blockIdx.x * 4 + 2] = xcc; stamps[blockIdx.x * 4 + 3] = hwid;
    }
  }
}
#define LAS __attribute__((address_space(3)))
typedef __attribute__((address_space(1))) unsigned long long gu64;
typedef __attribute__((ext_vector_type(4))) int i32x4;

#define WT(n) "s_waitcnt vmcnt(" #n ")\n\ts_branch 1f\n\t"
__device__ __forceinline__ void wait_tab(unsigned off) {
  asm volatile("s_getpc_b64 vcc\n\t"
               "s_add_u32 vcc_lo, vcc_lo, %0\n\t"
               "s_addc_u32 vcc_hi, vcc_hi, 0\n\t"
               "s_setpc_b64 vcc\n\t"
               WT(8) WT(9) WT(10) WT(11) WT(12) WT(13) WT(14) WT(15) WT(16) WT(17) WT(18) WT(19) WT(20) WT(21)
               WT(22) WT(23) WT(24) WT(25) WT(26) WT(27) WT(28) WT(29) WT(30) WT(31) WT(32)
               "1:"
               :: "s"(off) : "vcc", "scc", "memory");
}
__device__ __forceinline__ void dma_tile(u32x4 rs, unsigned voff, unsigned m0v, unsigned so) {
  asm volatile("s_mov_b32 m0, %0\n\ts_nop 0\n\t"
               "buffer_load_dwordx4 %1, %2, %3 offen nt lds\n\t"
               "buffer_load_dwordx4 %1, %2, %3 offen offset:1024 nt lds\n\t"
               "buffer_load_dwordx4 %1, %2, %3 offen offset:2048 nt lds\n\t"
               "buffer_load_dwordx4 %1, %2, %3 offen offset:3072 nt lds"
               :: "s"(m0v), "v"(voff), "s"(rs), "s"(so) : "memory");
}
template <int W> __device__ __forceinline__ void waitvm4() { asm volatile("s_waitcnt vmcnt(%0)" :: "n"(W) : "memory"); }
constexpr int NSH4 = 8;

template <int NRT, int R0, int RLO, int RHI, int NCP, int KP, int KI, int RD, int REND, int DBG = 0, int SLP = 1>
__global__ void __launch_bounds__(384) attn_4(const uint16_t* __restrict__ q, const uint16_t* __restrict__ k,
                                              const uint16_t* __restrict__ v, uint16_t* __restrict__ out, u64* __restrict__ ctl, u64* stamps) {
  u64 t0 = 0;
  if (stamps) t0 = __builtin_amdgcn_s_memrealtime();
  constexpr int NC = 4, NT = 1024;
  constexpr int RINGB = NRT * 4096, SCB = RINGB, MRGB = SCB + SK * 4, LNDB = MRGB + NC * (D + 4) * 4, FREEB = LNDB + 256, RCB = FREEB + 16, LDSB = RCB + 16;
  static_assert(NRT % 4 == 0 && RLO >= 12 && RHI <= 36 && (RHI + 3) / 4 + 4 < NRT && LDSB <= 163840 && NCP <= 64, "");
  __shared__ __attribute__((aligned(16))) char lds_[LDSB];
  LAS char* lds = (LAS char*)lds_;
  volatile LAS int* landed = (volatile LAS int*)(lds + LNDB);
  volatile LAS int* freew = (volatile LAS int*)(lds + FREEB);
  volatile LAS int* rctl = (volatile LAS int*)(lds + RCB);
  const int bh = blockIdx.x;
  const int tid = threadIdx.x, lane = tid & 63, wave = __builtin_amdgcn_readfirstlane(tid >> 6);
  constexpr float C2 = 0.088388347648318440f * 1.4426950408889634f;

  if (wave == 0) {
    if (!(DBG & 4)) __builtin_amdgcn_s_setprio(3);
    const u64 ka = (u64)(k + (size_t)bh * SK * D), va = (u64)(v + (size_t)bh * SK * D);
    const u32x4 krs = {(unsigned)ka, (unsigned)(ka >> 32) & 0xffffu, HEADB, 0x20000u};
    const u32x4 vrs = {(unsigned)va, (unsigned)(va >> 32) & 0xffffu, HEADB, 0x20000u};
    const unsigned voff = lane * 16;
    const unsigned ldsb = (unsigned)(size_t)lds;
#pragma unroll
    for (int t = 0; t < 4; ++t) dma_tile(krs, voff, ldsb + t * 4096, t * 4096);
    landed[lane] = 0;
    if (lane < 4) freew[lane] = 0;
    if (lane == 0) rctl[0] = R0;
    asm volatile("s_waitcnt lgkmcnt(0)" ::: "memory");
    __builtin_amdgcn_s_barrier();
    unsigned slot = 4 * 4096;
    int pub = 0;
    for (int ch = 1; ch < NT / 4; ++ch) {
      const int need = ch - NRT / 4 + 1;
      int R = R0;
      for (int sp = 0; sp < (1 << 22); ++sp) {
        const i32x4 f = *(volatile LAS i32x4*)freew;
        const int rn = rctl[0];
        const int mn = __builtin_amdgcn_readfirstlane(min(min(f[0], f[1]), min(f[2], f[3])));
        R = __builtin_amdgcn_readfirstlane(min(max(rn, RLO), RHI));
        if (mn >= need) break;
        waitvm4<0>();
        pub = ch * 4;
        landed[lane] = pub;
        __builtin_amdgcn_s_sleep(1);
      }
      if (REND && ch >= NT / 4 - REND) R = RHI;
      const unsigned off = 12 + 8 * (R - 4 - 8);
      const int lt = (R - 4 + 3) >> 2;
      const u32x4 rs = ch < NT / 8 ? krs : vrs;
      const unsigned so = (unsigned)(ch * 16384) & (HEADB - 1);
#pragma unroll
      for (int t = 0; t < 4; ++t) {
        if (DBG & 1) waitvm4<R0 - 4>(); else wait_tab(off);
        dma_tile(rs, voff, ldsb + slot + t * 4096, so + t * 4096);
        pub = max(pub, ch * 4 + t - lt);
        landed[lane] = pub;
      }
      slot += 16384; if (slot == RINGB) slot = 0;
    }
#define DRAIN(x) { waitvm4<x>(); pub = max(pub, NT - (x) / 4); landed[lane] = pub; }
    DRAIN(32) DRAIN(28) DRAIN(24) DRAIN(20) DRAIN(16) DRAIN(12) DRAIN(8) DRAIN(4) DRAIN(0)
#undef DRAIN
    __builtin_amdgcn_s_barrier();
  } else if (wave == 5) {
    const int sh = (bh >> 3) & (NSH4 - 1);
    gu64* cw = (gu64*)ctl;
    u64 basev = 0;
    if (KP) asm volatile("global_load_dwordx2 %0, %1, off sc1" : "=v"(basev) : "v"(cw + ((lane & (NCP - 1)) * NSH4 + ((sh + lane) & (NSH4 - 1))) * 32) : "memory");
    __builtin_amdgcn_s_barrier();
    if (KP) {
      asm volatile("s_waitcnt vmcnt(0)" : "+v"(basev) :: "memory");
      float integ = 0.f;
      for (int cp = 1; cp < NCP; ++cp) {
        for (int sp = 0; sp < (1 << 22) && __builtin_amdgcn_readfirstlane(landed[0]) < cp * (NT / NCP); ++sp) __builtin_amdgcn_s_sleep(2);
        const unsigned town = (unsigned)__builtin_amdgcn_s_memrealtime();
        const u64 addv = (1ull << 40) + town;
        if (lane == 0) asm volatile("global_atomic_add_x2 %0, %1, off" :: "v"(cw + (cp * NSH4 + sh) * 32), "v"(addv) : "memory");
        unsigned trd;
        do { __builtin_amdgcn_s_sleep(4); trd = (unsigned)__builtin_amdgcn_s_memrealtime(); } while (trd - town < RD);
        u64 rdv;
        asm volatile("global_load_dwordx2 %0, %1, off sc1\n\ts_waitcnt vmcnt(0)" : "=v"(rdv) : "v"(cw + (cp * NSH4 + ((sh + cp) & (NSH4 - 1))) * 32) : "memory");
        const unsigned blo = __builtin_amdgcn_readlane((unsigned)basev, cp), bhi = __builtin_amdgcn_readlane((unsigned)(basev >> 32), cp);
        const u64 d = rdv - (((u64)bhi << 32) | blo);
        const unsigned cnt = min((unsigned)(d >> 40), 32u);
        const int rel = (int)((unsigned)d - cnt * town) + (int)((32u - cnt) * (trd - town));
        const float lag = (float)rel * (-1.f / 32.f);
        if (KI) integ = fminf(fmaxf(integ + lag * (1.f / KI), (float)(RLO - R0)), (float)(RHI - R0));
        const float rf = fminf(fmaxf((float)R0 + lag * (1.f / KP) + integ, (float)RLO), (float)RHI);
        const int R = (int)rintf(rf);
        rctl[0] = R;
        if (stamps && lane == 0) stamps[4096 + bh * 64 + cp] = ((u64)(unsigned)R << 32) | (unsigned)(int)lag;
      }
    }
    asm volatile("s_waitcnt lgkmcnt(0)" ::: "memory");
    __builtin_amdgcn_s_barrier();
  } else {
    const int w = wave - 1, c = lane & 15, r = lane >> 4;
    const u32x4 qp = *reinterpret_cast<const u32x4*>(q + bh * D + 8 * c);
    __builtin_amdgcn_s_barrier();
    unsigned slot = w * 4096;
    LAS f32x4* sc = (LAS f32x4*)(lds + SCB) + w * 512 + r;
    int seen = 0;
    float m = -INFINITY;
    u32x4 x[4];
#define FETCH(j)                                                                                        \
    { const int t = 4 * (j) + w;                                                                        \
      for (int sp = 0; seen <= t && sp < (1 << 18); ++sp) { seen = __builtin_amdgcn_readfirstlane(landed[0]); if (seen <= t) __builtin_amdgcn_s_sleep(SLP); } \
      asm volatile("" ::: "memory");                                                                    \
      const LAS u32x4* src = (const LAS u32x4*)(lds + slot) + lane;                                     \
      _Pragma("unroll") for (int i = 0; i < 4; ++i) x[i] = (DBG & 2) ? qp : src[i * 64];                                 \
      asm volatile("s_waitcnt lgkmcnt(0)" : "+v"(x[0]), "+v"(x[1]), "+v"(x[2]), "+v"(x[3]) :: "memory"); \
      if (lane == 0) freew[w] = (j) + 1;                                                                \
      slot += 16384; if (slot >= RINGB) slot -= RINGB; }
    for (int j = 0; j < 128; ++j) {
      FETCH(j)
      f32x4 s;
#pragma unroll
      for (int i = 0; i < 4; ++i) {
        float d = dot2(x[i][0], qp[0], 0.f);
        d = dot2(x[i][1], qp[1], d); d = dot2(x[i][2], qp[2], d); d = dot2(x[i][3], qp[3], d);
        s[i] = row16_sum(d);
      }
      m = fmaxf(fmaxf(m, fmaxf(s[0], s[1])), fmaxf(s[2], s[3]));
      if (c == 0) sc[j * 4] = s;
    }
    const float mc = -m * C2;
    float l = 0.f;
    f32x2 acc[4];
#pragma unroll
    for (int i = 0; i < 4; ++i) acc[i] = f32x2{0.f, 0.f};
    for (int j = 0; j < 128; ++j) {
      f32x4 p = sc[j * 4];
#pragma unroll
      for (int i = 0; i < 4; ++i) p[i] = __builtin_amdgcn_exp2f(fmaf(p[i], C2, mc));
      l += (p[0] + p[1]) + (p[2] + p[3]);
      asm volatile("" : "+v"(p), "+v"(l));
      FETCH(128 + j)
#pragma unroll
      for (int i = 0; i < 4; ++i)
#pragma unroll
        for (int e = 0; e < 4; ++e) {
          const f32x2 vv = {__builtin_bit_cast(float, x[i][e] << 16), __builtin_bit_cast(float, x[i][e] & 0xffff0000u)};
          acc[e] = __builtin_elementwise_fma(f32x2{p[i], p[i]}, vv, acc[e]);
        }
    }
#undef FETCH
    const float M = xrow_max(m);
    const float wg = __builtin_amdgcn_exp2f((m - M) * C2);
    l = xrow_sum(l * wg);
    float a[8];
#pragma unroll
    for (int i = 0; i < 4; ++i) { a[2 * i] = xrow_sum(acc[i][0] * wg); a[2 * i + 1] = xrow_sum(acc[i][1] * wg); }
    LAS float* mrg = (LAS float*)(lds + MRGB);
    LAS float* rec = mrg + w * (D + 4);
    if (r == 0) {
      *(LAS f32x4*)(rec + 4 + 8 * c) = f32x4{a[0], a[1], a[2], a[3]};
      *(LAS f32x4*)(rec + 8 + 8 * c) = f32x4{a[4], a[5], a[6], a[7]};
      if (c == 0) { rec[0] = M; rec[1] = l; }
    }
    asm volatile("s_waitcnt lgkmcnt(0)" ::: "memory");
    __builtin_amdgcn_s_barrier();
    asm volatile("" ::: "memory");
    if (w == 0) {
      float MM = -INFINITY;
#pragma unroll
      for (int y = 0; y < NC; ++y) MM = fmaxf(MM, mrg[y * (D + 4)]);
      float Ls = 0.f, o0 = 0.f, o1 = 0.f;
#pragma unroll
      for (int y = 0; y < NC; ++y) {
        const LAS float* rx = mrg + y * (D + 4);
        const float wt = __builtin_amdgcn_exp2f((rx[0] - MM) * C2);
        Ls = fmaf(wt, rx[1], Ls);
        o0 = fmaf(wt, rx[4 + 2 * lane], o0);
        o1 = fmaf(wt, rx[5 + 2 * lane], o1);
      }
      const float inv = 1.f / Ls;
      __hip_bfloat162 o;
      o.x = __float2bfloat16(o0 * inv); o.y = __float2bfloat16(o1 * inv);
      reinterpret_cast<__hip_bfloat162*>(out + bh * D)[lane] = o;
      if (stamps && lane == 0) {
        u64 t1 = __builtin_amdgcn_s_memrealtime();
        unsigned xcc; asm volatile("s_getreg_b32 %0, hwreg(HW_REG_XCC_ID)" : "=s"(xcc));
        unsigned hwid; asm volatile("s_getreg_b32 %0, hwreg(HW_REG_HW_ID)" : "=s"(hwid));
        stamps[blockIdx.x * 4] = t0; stamps[blockIdx.x * 4 + 1] = t1; stamps[blockIdx.x * 4 + 2] = xcc; stamps[blockIdx.x * 4 + 3] = hwid;
      }
    }
  }
}

#define LV(n) "2:\n\ts_waitcnt vmcnt(" #n ")\n\t"                                   \
  "buffer_load_dwordx4 %[vo], %[rs], %[so] offen nt lds\n\t"                         \
  "buffer_load_dwordx4 %[vo], %[rs], %[so] offen offset:1024 nt lds\n\t"             \
  "buffer_load_dwordx4 %[vo], %[rs], %[so] offen offset:2048 nt lds\n\t"             \
  "buffer_load_dwordx4 %[vo], %[rs], %[so] offen offset:3072 nt lds\n\t"             \
  "s_add_u32 %[so], %[so], %[k4]\n\t"                                                \
  "s_add_u32 m0, m0, %[k4]\n\t"                                                      \
  "s_cmp_lg_u32 %[so], %[se]\n\t"                                                    \
  "s_cbranch_scc1 2b\n\t"                                                            \
  "s_branch 1f\n\t"
__device__ __forceinline__ void dma_group(u32x4 rs, unsigned voff, unsigned m0v, unsigned so, unsigned se, unsigned off) {
  const unsigned k4 = 4096;
  asm volatile("s_mov_b32 m0, %[m0v]\n\t"
               "s_getpc_b64 vcc\n\t"
               "s_add_u32 vcc_lo, vcc_lo, %[off]\n\t"
               "s_addc_u32 vcc_hi, vcc_hi, 0\n\t"
               "s_setpc_b64 vcc\n\t"
               LV(8) LV(9) LV(10) LV(11) LV(12) LV(13) LV(14) LV(15) LV(16) LV(17) LV(18) LV(19) LV(20) LV(21) LV(22) LV(23) LV(24)
               LV(25) LV(26) LV(27) LV(28)
               "1:"
               : [so] "+&s"(so)
               : [m0v] "s"(m0v), [vo] "v"(voff), [rs] "s"(rs), [se] "s"(se), [off] "s"(off), [k4] "s"(k4)
               : "vcc", "scc", "memory");
}

template <int G, int R0, int RLO, int RHI, int NCP, int KP, int KI, int RDL, int WDL, int REND, int DBG = 0>
__global__ void __launch_bounds__(384) attn_5(const uint16_t* __restrict__ q, const uint16_t* __restrict__ k,
                                              const uint16_t* __restrict__ v, uint16_t* __restrict__ out, u64* __restrict__ ctl, u64* stamps) {
  u64 t0 = 0;
  if (stamps) t0 = __builtin_amdgcn_s_memrealtime();
  constexpr int NC = 4, NT = 1024, NG = NT / G, LAGG = 8 / G;
  constexpr int NRG = LAGG + 2, GB = G * 4096, RINGB = NRG * GB;
  constexpr int SCB = RINGB, MRGB = SCB + SK * 4, RCB = MRGB + NC * (D + 4) * 4, LDSB = RCB + 16;
  constexpr int CPI = NG / NCP;
  static_assert((G == 4 || G == 8) && RLO >= 12 && RHI <= 32 && LDSB <= 163840 && NCP <= 64 && RDL + WDL <= CPI, "");
  __shared__ __attribute__((aligned(16))) char lds_[LDSB];
  LAS char* lds = (LAS char*)lds_;
  volatile LAS int* rctl = (volatile LAS int*)(lds + RCB);
  const int bh = blockIdx.x;
  const int tid = threadIdx.x, lane = tid & 63, wave = __builtin_amdgcn_readfirstlane(tid >> 6);
  constexpr float C2 = 0.088388347648318440f * 1.4426950408889634f;

  if (wave == 0) {
    __builtin_amdgcn_s_setprio(3);
    const u64 ka = (u64)(k + (size_t)bh * SK * D), va = (u64)(v + (size_t)bh * SK * D);
    const u32x4 krs = {(unsigned)ka, (unsigned)(ka >> 32) & 0xffffu, HEADB, 0x20000u};
    const u32x4 vrs = {(unsigned)va, (unsigned)(va >> 32) & 0xffffu, HEADB, 0x20000u};
    const unsigned voff = lane * 16;
    const unsigned ldsb = (unsigned)(size_t)lds;
    unsigned slot = 0;
    int R = R0;
    for (int g = 0; g < NG; ++g) {
      int rn = R0;
      if (KP && g > 0) rn = rctl[0];
      const u32x4 rs = g < NG / 2 ? krs : vrs;
      const unsigned so = (unsigned)(g * GB) & (HEADB - 1);
      dma_group(rs, voff, ldsb + slot, so, so + GB, 12 + 56 * (R - 4 - 8));
      __builtin_amdgcn_s_barrier();
      R = __builtin_amdgcn_readfirstlane(min(max(rn, RLO), RHI));
      if (REND && g >= NG - 1 - REND) R = RHI;
      slot += GB; if (slot == RINGB) slot = 0;
    }
    waitvm4<16>();
    __builtin_amdgcn_s_barrier();
    waitvm4<0>();
    __builtin_amdgcn_s_barrier();
    __builtin_amdgcn_s_barrier();
  } else if (wave == 5) {
    const int sh = (bh >> 3) & (NSH4 - 1);
    gu64* cw = (gu64*)ctl;
    u64 basev = 0;
    rctl[0] = R0;
    if (KP) asm volatile("global_load_dwordx2 %0, %1, off sc1" : "=v"(basev) : "v"(cw + ((lane & (NCP - 1)) * NSH4 + ((sh + lane) & (NSH4 - 1))) * 32) : "memory");
    asm volatile("s_waitcnt lgkmcnt(0)" ::: "memory");
    auto bars = [&](int n) { for (int i = 0; i < n; ++i) __builtin_amdgcn_s_barrier(); };
    bars(CPI);
    asm volatile("s_waitcnt vmcnt(0)" : "+v"(basev) :: "memory");
    float integ = 0.f;
    for (int cp = 1; cp < NCP; ++cp) {
      if (!KP) { bars(CPI); continue; }
      const unsigned town = (unsigned)__builtin_amdgcn_s_memrealtime();
      const u64 addv = (1ull << 40) + town;
      if (!(DBG & 1) && lane == 0) asm volatile("global_atomic_add_x2 %0, %1, off" :: "v"(cw + (cp * NSH4 + sh) * 32), "v"(addv) : "memory");
      bars(RDL);
      const unsigned trd = (unsigned)__builtin_amdgcn_s_memrealtime();
      u64 rdv = 0;
      const int cr = (DBG & 4) ? cp - 1 : cp;
      if (!(DBG & 2)) {
        if (DBG & 8) { if (lane == 0) asm volatile("global_load_dwordx2 %0, %1, off sc1" : "=v"(rdv) : "v"(cw + (cr * NSH4 + ((sh + cr) & (NSH4 - 1))) * 32) : "memory"); }
        else asm volatile("global_load_dwordx2 %0, %1, off sc1" : "=v"(rdv) : "v"(cw + (cr * NSH4 + ((sh + cr) & (NSH4 - 1))) * 32) : "memory");
      }
      bars(WDL);
      asm volatile("s_waitcnt vmcnt(0)" : "+v"(rdv) :: "memory");
      const unsigned blo = __builtin_amdgcn_readlane((unsigned)basev, cp), bhi = __builtin_amdgcn_readlane((unsigned)(basev >> 32), cp);
      const u64 d = rdv - (((u64)bhi << 32) | blo);
      const unsigned cnt = min((unsigned)(d >> 40), 32u);
      const int rel = (int)((unsigned)d - cnt * town) + (int)((32u - cnt) * (trd - town));
      const float lag = (float)rel * (-1.f / 32.f);
      if (KI) integ = fminf(fmaxf(integ + lag * (1.f / KI), (float)(RLO - R0)), (float)(RHI - R0));
      const float rf = fminf(fmaxf((float)R0 + lag * (1.f / KP) + integ, (float)RLO), (float)RHI);
      const int R = (int)rintf(rf);
      rctl[0] = R;
      if (stamps && lane == 0) stamps[4096 + bh * 64 + cp] = ((u64)(unsigned)R << 32) | (unsigned)(int)lag;
      asm volatile("s_waitcnt lgkmcnt(0)" ::: "memory");
      bars(CPI - RDL - WDL);
    }
    bars(3);
  } else {
    const int w = wave - 1, c = lane & 15, r = lane >> 4;
    const u32x4 qp = *reinterpret_cast<const u32x4*>(q + bh * D + 8 * c);
    unsigned slot = w * 4096;
    LAS f32x4* sc = (LAS f32x4*)(lds + SCB) + w * 512 + r;
    float m = -INFINITY;
    u32x4 x[4];
#pragma unroll
    for (int i = 0; i < LAGG; ++i) __builtin_amdgcn_s_barrier();
#define FETCH(BAR)                                                                                      \
    { if (BAR) { asm volatile("s_waitcnt lgkmcnt(0)" ::: "memory"); __builtin_amdgcn_s_barrier(); }     \
      asm volatile("" ::: "memory");                                                                    \
      const LAS u32x4* src = (const LAS u32x4*)(lds + slot) + lane;                                     \
      _Pragma("unroll") for (int i = 0; i < 4; ++i) x[i] = src[i * 64];                                 \
      slot += 16384; if (slot >= RINGB) slot -= RINGB; }
    for (int j = 0; j < 128; ++j) {
      FETCH(G == 4 || (j & 1) == 0)
      f32x4 s;
#pragma unroll
      for (int i = 0; i < 4; ++i) {
        float d = dot2(x[i][0], qp[0], 0.f);
        d = dot2(x[i][1], qp[1], d); d = dot2(x[i][2], qp[2], d); d = dot2(x[i][3], qp[3], d);
        s[i] = row16_sum(d);
      }
      m = fmaxf(fmaxf(m, fmaxf(s[0], s[1])), fmaxf(s[2], s[3]));
      if (c == 0) sc[j * 4] = s;
    }
    const float mc = -m * C2;
    float l = 0.f;
    f32x2 acc[4];
#pragma unroll
    for (int i = 0; i < 4; ++i) acc[i] = f32x2{0.f, 0.f};
    for (int j = 0; j < 128; ++j) {
      f32x4 p = sc[j * 4];
#pragma unroll
      for (int i = 0; i < 4; ++i) p[i] = __builtin_amdgcn_exp2f(fmaf(p[i], C2, mc));
      l += (p[0] + p[1]) + (p[2] + p[3]);
      asm volatile("" : "+v"(p), "+v"(l));
      FETCH(G == 4 || (j & 1) == 0 || j == 127)
#pragma unroll
      for (int i = 0; i < 4; ++i)
#pragma unroll
        for (int e = 0; e < 4; ++e) {
          const f32x2 vv = {__builtin_bit_cast(float, x[i][e] << 16), __builtin_bit_cast(float, x[i][e] & 0xffff0000u)};
          acc[e] = __builtin_elementwise_fma(f32x2{p[i], p[i]}, vv, acc[e]);
        }
    }
#undef FETCH
    const float M = xrow_max(m);
    const float wg = __builtin_amdgcn_exp2f((m - M) * C2);
    l = xrow_sum(l * wg);
    float a[8];
#pragma unroll
    for (int i = 0; i < 4; ++i) { a[2 * i] = xrow_sum(acc[i][0] * wg); a[2 * i + 1] = xrow_sum(acc[i][1] * wg); }
    LAS float* mrg = (LAS float*)(lds + MRGB);
    LAS float* rec = mrg + w * (D + 4);
    if (r == 0) {
      *(LAS f32x4*)(rec + 4 + 8 * c) = f32x4{a[0], a[1], a[2], a[3]};
      *(LAS f32x4*)(rec + 8 + 8 * c) = f32x4{a[4], a[5], a[6], a[7]};
      if (c == 0) { rec[0] = M; rec[1] = l; }
    }
    asm volatile("s_waitcnt lgkmcnt(0)" ::: "memory");
    __builtin_amdgcn_s_barrier();
    asm volatile("" ::: "memory");
    if (w == 0) {
      float MM = -INFINITY;
#pragma unroll
      for (int y = 0; y < NC; ++y) MM = fmaxf(MM, mrg[y * (D + 4)]);
      float Ls = 0.f, o0 = 0.f, o1 = 0.f;
#pragma unroll
      for (int y = 0; y < NC; ++y) {
        const LAS float* rx = mrg + y * (D + 4);
        const float wt = __builtin_amdgcn_exp2f((rx[0] - MM) * C2);
        Ls = fmaf(wt, rx[1], Ls);
        o0 = fmaf(wt, rx[4 + 2 * lane], o0);
        o1 = fmaf(wt, rx[5 + 2 * lane], o1);
      }
      const float inv = 1.f / Ls;
      __hip_bfloat162 o;
      o.x = __float2bfloat16(o0 * inv); o.y = __float2bfloat16(o1 * inv);
      reinterpret_cast<__hip_bfloat162*>(out + bh * D)[lane] = o;
      if (stamps && lane == 0) {
        u64 t1 = __builtin_amdgcn_s_memrealtime();
        unsigned xcc; asm volatile("s_getreg_b32 %0, hwreg(HW_REG_XCC_ID)" : "=s"(xcc));
        unsigned hwid; asm volatile("s_getreg_b32 %0, hwreg(HW_REG_HW_ID)" : "=s"(hwid));
        stamps[blockIdx.x * 4] = t0; stamps[blockIdx.x * 4 + 1] = t1; stamps[blockIdx.x * 4 + 2] = xcc; stamps[blockIdx.x * 4 + 3] = hwid;
      }
    }
  }
}


template <int FL> __device__ __forceinline__ u64 ctl_load(gu64* p) {
  u64 r;
  if (FL == 0) asm volatile("global_load_dwordx2 %0, %1, off sc1" : "=v"(r) : "v"(p) : "memory");
  else if (FL == 1) asm volatile("global_load_dwordx2 %0, %1, off sc0 sc1" : "=v"(r) : "v"(p) : "memory");
  else if (FL == 2) asm volatile("global_load_dwordx2 %0, %1, off sc1 nt" : "=v"(r) : "v"(p) : "memory");
  else if (FL == 3) asm volatile("global_load_dwordx2 %0, %1, off sc0 sc1 nt" : "=v"(r) : "v"(p) : "memory");
  else { const u64 z = 0; asm volatile("global_atomic_add_x2 %0, %1, %2, off sc0" : "=&v"(r) : "v"(p), "v"(z) : "memory"); }
  return r;
}
template <int G, int R0, int RLO, int RHI, int NCP, int KP, int KI, int RDL, int WDL, int REND, int NPUB, int DBG = 0>
__global__ void __launch_bounds__(384) attn_6(const uint16_t* __restrict__ q, const uint16_t* __restrict__ k,
                                              const uint16_t* __restrict__ v, uint16_t* __restrict__ out, u64* __restrict__ ctl, u64* stamps) {
  u64 t0 = 0;
  if (stamps) t0 = __builtin_amdgcn_s_memrealtime();
  constexpr int NC = 4, NT = 1024, NG = NT / G, LAGG = 8 / G;
  constexpr int NRG = LAGG + 2, GB = G * 4096, RINGB = NRG * GB;
  constexpr int SCB = RINGB, MRGB = SCB + SK * 4, RCB = MRGB + NC * (D + 4) * 4, LDSB = RCB + 16;
  constexpr int CPI = NG / NCP;
  static_assert((G == 4 || G == 8) && RLO >= 12 && RHI <= 32 && LDSB <= 163840 && NCP <= 64 && RDL + WDL <= CPI, "");
  __shared__ __attribute__((aligned(16))) char lds_[LDSB];
  LAS char* lds = (LAS char*)lds_;
  volatile LAS int* rctl = (volatile LAS int*)(lds + RCB);
  const int bh = blockIdx.x;
  const int tid = threadIdx.x, lane = tid & 63, wave = __builtin_amdgcn_readfirstlane(tid >> 6);
  constexpr float C2 = 0.088388347648318440f * 1.4426950408889634f;

  if (wave == 0) {
    __builtin_amdgcn_s_setprio(3);
    const u64 ka = (u64)(k + (size_t)bh * SK * D), va = (u64)(v + (size_t)bh * SK * D);
    const u32x4 krs = {(unsigned)ka, (unsigned)(ka >> 32) & 0xffffu, HEADB, 0x20000u};
    const u32x4 vrs = {(unsigned)va, (unsigned)(va >> 32) & 0xffffu, HEADB, 0x20000u};
    const unsigned voff = lane * 16;
    const unsigned ldsb = (unsigned)(size_t)lds;
    unsigned slot = 0;
    int R = R0;
    for (int g = 0; g < NG; ++g) {
      int rn = R0;
      if (KP && g > 0) rn = rctl[0];
      const u32x4 rs = g < NG / 2 ? krs : vrs;
      const unsigned so = (unsigned)(g * GB) & (HEADB - 1);
      dma_group(rs, voff, ldsb + slot, so, so + GB, 12 + 56 * (R - 4 - 8));
      __builtin_amdgcn_s_barrier();
      R = __builtin_amdgcn_readfirstlane(min(max(rn, RLO), RHI));
      if (REND && g >= NG - 1 - REND) R = RHI;
      slot += GB; if (slot == RINGB) slot = 0;
    }
    waitvm4<16>();
    __builtin_amdgcn_s_barrier();
    waitvm4<0>();
    __builtin_amdgcn_s_barrier();
    __builtin_amdgcn_s_barrier();
  } else if (wave == 5) {
    constexpr int STR = 256 / NPUB;
    const int idx = bh >> 3;
    const bool pubr = (idx % STR) == ((idx / STR) % STR);
    gu64* cw = (gu64*)ctl;
    constexpr int CST = 512 + 32;
    u64 basev = 0;
    rctl[0] = R0;
    if (KP && lane < NCP) basev = ctl_load<(DBG & 7)>(cw + lane * CST);
    asm volatile("s_waitcnt lgkmcnt(0)" ::: "memory");
    auto bars = [&](int n) { for (int i = 0; i < n; ++i) __builtin_amdgcn_s_barrier(); };
    bars(CPI);
    asm volatile("s_waitcnt vmcnt(0)" : "+v"(basev) :: "memory");
    float integ = 0.f;
    for (int cp = 1; cp < NCP; ++cp) {
      if (!KP) { bars(CPI); continue; }
      const unsigned town = (unsigned)__builtin_amdgcn_s_memrealtime();
      const u64 addv = (1ull << 40) + town;
      if (pubr && lane == 0) { if (DBG & 8) asm volatile("global_atomic_add_x2 %0, %1, off sc1" :: "v"(cw + cp * CST), "v"(addv) : "memory");
        else asm volatile("global_atomic_add_x2 %0, %1, off" :: "v"(cw + cp * CST), "v"(addv) : "memory"); }
      bars(RDL);
      const unsigned trd = (unsigned)__builtin_amdgcn_s_memrealtime();
      u64 rdv = 0;
      if (lane == 0) rdv = ctl_load<(DBG & 7)>(cw + cp * CST);
      bars(WDL);
      asm volatile("s_waitcnt vmcnt(0)" : "+v"(rdv) :: "memory");
      const unsigned blo = __builtin_amdgcn_readlane((unsigned)basev, cp), bhi = __builtin_amdgcn_readlane((unsigned)(basev >> 32), cp);
      const u64 d = rdv - (((u64)bhi << 32) | blo);
      const unsigned cnt = min((unsigned)(d >> 40), (unsigned)NPUB);
      const int rel = (int)((unsigned)d - cnt * town) + (int)((NPUB - cnt) * (trd - town));
      const float lag = (float)rel * (-1.f / NPUB);
      if (KI) integ = fminf(fmaxf(integ + lag * (1.f / KI), (float)(RLO - R0)), (float)(RHI - R0));
      const float rf = fminf(fmaxf((float)R0 + lag * (1.f / KP) + integ, (float)RLO), (float)RHI);
      const int R = (int)rintf(rf);
      if (lane == 0) rctl[0] = R;
      if ((DBG & 16) && stamps && lane == 0) stamps[4096 + bh * 64 + cp] = ((u64)(unsigned)R << 32) | (unsigned)(int)lag;
      asm volatile("s_waitcnt lgkmcnt(0)" ::: "memory");
      bars(CPI - RDL - WDL);
    }
    bars(3);
  } else {
    const int w = wave - 1, c = lane & 15, r = lane >> 4;
    const u32x4 qp = *reinterpret_cast<const u32x4*>(q + bh * D + 8 * c);
    unsigned slot = w * 4096;
    LAS f32x4* sc = (LAS f32x4*)(lds + SCB) + w * 512 + r;
    float m = -INFINITY;
    u32x4 x[4];
#pragma unroll
    for (int i = 0; i < LAGG; ++i) __builtin_amdgcn_s_barrier();
#define FETCH(BAR)                                                                                      \
    { if (BAR) { asm volatile("s_waitcnt lgkmcnt(0)" ::: "memory"); __builtin_amdgcn_s_barrier(); }     \
      asm volatile("" ::: "memory");                                                                    \
      const LAS u32x4* src = (const LAS u32x4*)(lds + slot) + lane;                                     \
      _Pragma("unroll") for (int i = 0; i < 4; ++i) x[i] = src[i * 64];                                 \
      slot += 16384; if (slot >= RINGB) slot -= RINGB; }
    for (int j = 0; j < 128; ++j) {
      FETCH(G == 4 || (j & 1) == 0)
      f32x4 s;
#pragma unroll
      for (int i = 0; i < 4; ++i) {
        float d = dot2(x[i][0], qp[0], 0.f);
        d = dot2(x[i][1], qp[1], d); d = dot2(x[i][2], qp[2], d); d = dot2(x[i][3], qp[3], d);
        s[i] = row16_sum(d);
      }
      m = fmaxf(fmaxf(m, fmaxf(s[0], s[1])), fmaxf(s[2], s[3]));
      if (c == 0) sc[j * 4] = s;
    }
    const float mc = -m * C2;
    float l = 0.f;
    f32x2 acc[4];
#pragma unroll
    for (int i = 0; i < 4; ++i) acc[i] = f32x2{0.f, 0.f};
    for (int j = 0; j < 128; ++j) {
      f32x4 p = sc[j * 4];
#pragma unroll
      for (int i = 0; i < 4; ++i) p[i] = __builtin_amdgcn_exp2f(fmaf(p[i], C2, mc));
      l += (p[0] + p[1]) + (p[2] + p[3]);
      asm volatile("" : "+v"(p), "+v"(l));
      FETCH(G == 4 || (j & 1) == 0 || j == 127)
#pragma unroll
      for (int i = 0; i < 4; ++i)
#pragma unroll
        for (int e = 0; e < 4; ++e) {
          const f32x2 vv = {__builtin_bit_cast(float, x[i][e] << 16), __builtin_bit_cast(float, x[i][e] & 0xffff0000u)};
          acc[e] = __builtin_elementwise_fma(f32x2{p[i], p[i]}, vv, acc[e]);
        }
    }
#undef FETCH
    const float M = xrow_max(m);
    const float wg = __builtin_amdgcn_exp2f((m - M) * C2);
    l = xrow_sum(l * wg);
    float a[8];
#pragma unroll
    for (int i = 0; i < 4; ++i) { a[2 * i] = xrow_sum(acc[i][0] * wg); a[2 * i + 1] = xrow_sum(acc[i][1] * wg); }
    LAS float* mrg = (LAS float*)(lds + MRGB);
    LAS float* rec = mrg + w * (D + 4);
    if (r == 0) {
      *(LAS f32x4*)(rec + 4 + 8 * c) = f32x4{a[0], a[1], a[2], a[3]};
      *(LAS f32x4*)(rec + 8 + 8 * c) = f32x4{a[4], a[5], a[6], a[7]};
      if (c == 0) { rec[0] = M; rec[1] = l; }
    }
    asm volatile("s_waitcnt lgkmcnt(0)" ::: "memory");
    __builtin_amdgcn_s_barrier();
    asm volatile("" ::: "memory");
    if (w == 0) {
      float MM = -INFINITY;
#pragma unroll
      for (int y = 0; y < NC; ++y) MM = fmaxf(MM, mrg[y * (D + 4)]);
      float Ls = 0.f, o0 = 0.f, o1 = 0.f;
#pragma unroll
      for (int y = 0; y < NC; ++y) {
        const LAS float* rx = mrg + y * (D + 4);
        const float wt = __builtin_amdgcn_exp2f((rx[0] - MM) * C2);
        Ls = fmaf(wt, rx[1], Ls);
        o0 = fmaf(wt, rx[4 + 2 * lane], o0);
        o1 = fmaf(wt, rx[5 + 2 * lane], o1);
      }
      const float inv = 1.f / Ls;
      __hip_bfloat162 o;
      o.x = __float2bfloat16(o0 * inv); o.y = __float2bfloat16(o1 * inv);
      reinterpret_cast<__hip_bfloat162*>(out + bh * D)[lane] = o;
      if (stamps && lane == 0) {
        u64 t1 = __builtin_amdgcn_s_memrealtime();
        unsigned xcc; asm volatile("s_getreg_b32 %0, hwreg(HW_REG_XCC_ID)" : "=s"(xcc));
        unsigned hwid; asm volatile("s_getreg_b32 %0, hwreg(HW_REG_HW_ID)" : "=s"(hwid));
        stamps[blockIdx.x * 4] = t0; stamps[blockIdx.x * 4 + 1] = t1; stamps[blockIdx.x * 4 + 2] = xcc; stamps[blockIdx.x * 4 + 3] = hwid;
      }
    }
  }
}

extern "C" void kernel_launch(void* const* d_in, const int* in_sizes, int n_in, void* d_out, int out_size, void* d_ws,
                              size_t ws_size, hipStream_t stream) {
  const int BH = in_sizes[0] / D;
  attn_6<4,22,14,32,32,40,0,4,3,0,8,0><<<BH, 384, 0, stream>>>((const uint16_t*)d_in[0], (const uint16_t*)d_in[1], (const uint16_t*)d_in[2], (uint16_t*)d_out, (u64*)d_ws, nullptr);
}
```

```cpp
#include <hip/hip_runtime.h>
#include <hip/hip_bf16.h>
#include <stdint.h>
namespace v12 {
typedef unsigned long long u64;
using u32x4 = __attribute__((ext_vector_type(4))) unsigned;
using f32x4 = __attribute__((ext_vector_type(4))) float;
using f32x2 = __attribute__((ext_vector_type(2))) float;
using bf16x2 = __attribute__((ext_vector_type(2))) __bf16;
#define LAS __attribute__((address_space(3)))
typedef __attribute__((address_space(1))) unsigned long long gu64;
constexpr int SK = 8192, D = 128;
constexpr unsigned HEADB = SK * D * 2;

__device__ __forceinline__ float dot2(unsigned a, unsigned b, float acc) {
  return __builtin_amdgcn_fdot2_f32_bf16(__builtin_bit_cast(bf16x2, a), __builtin_bit_cast(bf16x2, b), acc, false);
}
template <int CTRL> __device__ __forceinline__ float dpp(float x) {
  return __builtin_bit_cast(float, __builtin_amdgcn_mov_dpp(__builtin_bit_cast(int, x), CTRL, 0xf, 0xf, true));
}
__device__ __forceinline__ float row16_sum(float x) {
  x += dpp<0xB1>(x); x += dpp<0x4E>(x); x += dpp<0x141>(x); x += dpp<0x128>(x); return x;
}
__device__ __forceinline__ float swap32_add(float a, float b) {
  auto t = __builtin_amdgcn_permlane32_swap(__float_as_uint(a), __float_as_uint(b), false, false);
  return __uint_as_float(t[0]) + __uint_as_float(t[1]);
}
__device__ __forceinline__ float swap16_add(float a, float b) {
  auto t = __builtin_amdgcn_permlane16_swap(__float_as_uint(a), __float_as_uint(b), false, false);
  return __uint_as_float(t[0]) + __uint_as_float(t[1]);
}
__device__ __forceinline__ float xrow_max(float x) {
  auto s = __builtin_amdgcn_permlane16_swap(__float_as_uint(x), __float_as_uint(x), false, false);
  x = fmaxf(__uint_as_float(s[0]), __uint_as_float(s[1]));
  auto t = __builtin_amdgcn_permlane32_swap(__float_as_uint(x), __float_as_uint(x), false, false);
  return fmaxf(__uint_as_float(t[0]), __uint_as_float(t[1]));
}
template <int W> __device__ __forceinline__ void waitvm() { asm volatile("s_waitcnt vmcnt(%0)" :: "n"(W) : "memory"); }
__device__ __forceinline__ void bar() { __builtin_amdgcn_s_barrier(); }
__device__ __forceinline__ void lds_bar() { asm volatile("s_waitcnt lgkmcnt(0)\n\ts_barrier" ::: "memory"); }

#define LV12(n) "2:\n\ts_waitcnt vmcnt(" #n ")\n\t"                                  \
  "buffer_load_dwordx4 %[vo], %[rs], %[so] offen nt lds\n\t"                         \
  "buffer_load_dwordx4 %[vo], %[rs], %[so] offen offset:1024 nt lds\n\t"             \
  "buffer_load_dwordx4 %[vo], %[rs], %[so] offen offset:2048 nt lds\n\t"             \
  "buffer_load_dwordx4 %[vo], %[rs], %[so] offen offset:3072 nt lds\n\t"             \
  "s_add_u32 %[so], %[so], %[k4]\n\ts_add_u32 m0, m0, %[k4]\n\ts_cmp_lg_u32 %[so], %[se]\n\ts_cbranch_scc1 2b\n\ts_branch 1f\n\t"
__device__ __forceinline__ void dma_group(u32x4 rs, unsigned voff, unsigned m0v, unsigned so, unsigned se, int W) {
  const unsigned k4 = 4096, off = 12 + 56 * (W - 12);
  asm volatile("s_mov_b32 m0, %[m0v]\n\ts_getpc_b64 vcc\n\ts_add_u32 vcc_lo, vcc_lo, %[off]\n\ts_addc_u32 vcc_hi, vcc_hi, 0\n\ts_setpc_b64 vcc\n\t"
               LV12(12) LV12(13) LV12(14) LV12(15) LV12(16) LV12(17) LV12(18) LV12(19) LV12(20) LV12(21) LV12(22) LV12(23) LV12(24) LV12(25) LV12(26) LV12(27) LV12(28)
               "1:"
               : [so] "+&s"(so) : [m0v] "s"(m0v), [vo] "v"(voff), [rs] "s"(rs), [se] "s"(se), [off] "s"(off), [k4] "s"(k4) : "vcc", "scc", "memory");
}

__device__ __forceinline__ void dma_burst(u32x4 rs, unsigned voff, unsigned m0v, unsigned so, unsigned se) {
  const unsigned k4 = 4096;
  asm volatile("s_mov_b32 m0, %[m0v]\n\ts_nop 0\n"
               "2:\n\t"
               "buffer_load_dwordx4 %[vo], %[rs], %[so] offen nt lds\n\t"
               "buffer_load_dwordx4 %[vo], %[rs], %[so] offen offset:1024 nt lds\n\t"
               "buffer_load_dwordx4 %[vo], %[rs], %[so] offen offset:2048 nt lds\n\t"
               "buffer_load_dwordx4 %[vo], %[rs], %[so] offen offset:3072 nt lds\n\t"
               "s_add_u32 %[so], %[so], %[k4]\n\ts_add_u32 m0, m0, %[k4]\n\ts_cmp_lg_u32 %[so], %[se]\n\ts_cbranch_scc1 2b"
               : [so] "+&s"(so) : [m0v] "s"(m0v), [vo] "v"(voff), [rs] "s"(rs), [se] "s"(se), [k4] "s"(k4) : "scc", "memory");
}
template <int NC, int W10, int WLO, int WHI, int NCP, int KP, int RDL, int WDL, int NPUB, int MODE, int FILT, int TB, int REND, int WEND, int DBG>
__global__ void __launch_bounds__((NC + 2) * 64) attn(const uint16_t* __restrict__ q, const uint16_t* __restrict__ k,
                                            const uint16_t* __restrict__ v, uint16_t* __restrict__ out, u64* __restrict__ ctl, u64* stamps) {
  u64 t0 = 0;
  if (DBG & 1) t0 = __builtin_amdgcn_s_memrealtime();
  constexpr int W0 = (W10 + 5) / 10, PRE = 5, ST = 0;
  constexpr float W0f = W10 * 0.1f;
  constexpr int NG = 256, GB = 16384, RINGB = 4 * GB;
  constexpr int LPW = 16 / NC;
  constexpr int SCB = RINGB, REDB = SCB + SK * 4, MXB = REDB + NC * 512, LSB = MXB + 32, RCB = LSB + 32, LDSB = RCB + 16;
  constexpr int CPI = NG / NCP;
  static_assert(WLO >= 12 && WHI <= 28 && NCP <= 64 && RDL + WDL <= CPI && CPI > 3, "");
  __shared__ __attribute__((aligned(16))) char lds_[LDSB];
  LAS char* lds = (LAS char*)lds_;
  volatile LAS int* rctl = (volatile LAS int*)(lds + RCB);
  const int bh = blockIdx.x;
  const int tid = threadIdx.x, lane = tid & 63, wave = __builtin_amdgcn_readfirstlane(tid >> 6);
  constexpr float C2 = 0.088388347648318440f * 1.4426950408889634f;

  if (__builtin_expect(wave == 0, 1)) {
    __builtin_amdgcn_s_setprio(3);
    const u64 ka = (u64)(k + (size_t)bh * SK * D), va = (u64)(v + (size_t)bh * SK * D);
    u32x4 rs = {(unsigned)ka, (unsigned)(ka >> 32) & 0xffffu, HEADB, 0x20000u};
    const unsigned voff = lane * 16;
    const unsigned ldsb = (unsigned)(size_t)lds;
    int W = W0;
    if (PRE) dma_burst(rs, voff, ldsb, 0, PRE * 4096);
    unsigned pos = PRE * 4096, len = 3 * GB - PRE * 4096;
    asm volatile("" : "+s"(W), "+s"(pos), "+s"(len));
    u64 tA = 0, tB = 0, tC = 0;
    if (DBG & 1) tA = __builtin_amdgcn_s_memrealtime();
#pragma unroll 1
    do {
      const int rn = rctl[0];
      const unsigned so = pos & (HEADB - 1);
      dma_group(rs, voff, ldsb + (so & (RINGB - 1)), so, so + len, W);
      bar();
      if (KP && len == GB) W = __builtin_amdgcn_readfirstlane(min(max(rn, WLO), WHI));
      pos += len; len = GB;
      if (REND && pos >= 2 * HEADB - REND * GB) W = WEND;
      if (pos == HEADB) { rs[0] = (unsigned)va; rs[1] = (unsigned)(va >> 32) & 0xffffu; }
    } while (pos != 2 * HEADB);
    if (DBG & 1) tB = __builtin_amdgcn_s_memrealtime();
    waitvm<16>();
    bar();
    waitvm<0>();
    if (DBG & 1) tC = __builtin_amdgcn_s_memrealtime();
    bar();
    bar();
    if ((DBG & 1) && stamps && lane == 0) { u64* s2 = stamps + 1024 + bh * 4; s2[0] = t0; s2[1] = tA; s2[2] = tB; s2[3] = tC; }
  } else if (wave == NC + 1) {
    rctl[0] = W0;
    constexpr int STR = 256 / NPUB;
    const int idx = bh >> 3;
    const bool pubr = (idx % STR) == ((idx / STR) % STR);
    gu64* cw = (gu64*)ctl;
    constexpr int CST = 512 + 32;
    u64 basev = 0;
    if (KP && lane < NCP) asm volatile("global_load_dwordx2 %0, %1, off sc1" : "=v"(basev) : "v"(cw + lane * CST) : "memory");
    unsigned tlog = 0, rlog = 0;
    unsigned tref = 0, tprev = 0, tnow = 0;
    float sch = 0.f, tau = 0.f, iv1 = 1e9f, fprev = 0.f, lagf = 0.f;
    bool on = false;
    int Wl = W0;
    auto step = [&]() {
      lds_bar();
      tnow = (unsigned)__builtin_amdgcn_s_memrealtime();
      if (MODE == 1) {
        if (on) {
          const float lag = (float)(int)(tnow - tref) - sch;
          lagf = FILT ? lagf + (lag - lagf) * (1.f / FILT) : lag;
          Wl = (int)rintf(fminf(fmaxf(W0f + lagf * (1.f / KP), (float)WLO), (float)WHI));
          rctl[0] = Wl;
        }
        sch += tau;
      }
    };
#pragma unroll 1
    for (int i = 2; i < CPI; ++i) step();
    asm volatile("s_waitcnt vmcnt(0)" : "+v"(basev) :: "memory");
#pragma unroll 1
    for (int cp = 1; cp < NCP; ++cp) {
      const unsigned town = tnow;
      if (DBG & 1) tlog = lane == cp ? town : tlog;
      const u64 addv = (1ull << 40) + town;
      if (KP && pubr && lane == 0) asm volatile("global_atomic_add_x2 %0, %1, off" :: "v"(cw + cp * CST), "v"(addv) : "memory");
#pragma unroll 1
      for (int i = 0; i < RDL; ++i) step();
      const unsigned trd = tnow;
      u64 rdv = 0;
      if (KP) asm volatile("global_load_dwordx2 %0, %1, off sc1" : "=v"(rdv) : "v"(cw + cp * CST) : "memory");
#pragma unroll 1
      for (int i = 0; i < WDL; ++i) step();
      if (KP) {
        asm volatile("s_waitcnt vmcnt(0)" : "+v"(rdv) :: "memory");
        const unsigned blo = __builtin_amdgcn_readlane((unsigned)basev, cp), bhi = __builtin_amdgcn_readlane((unsigned)(basev >> 32), cp);
        const u64 d = rdv - (((u64)bhi << 32) | blo);
        const unsigned cnt = min((unsigned)(d >> 40), (unsigned)NPUB);
        const int rel = (int)((unsigned)d - cnt * town) + (int)((NPUB - cnt) * (trd - town));
        const float frac = (float)rel * (1.f / NPUB);
        const float iv = (float)(int)(town - tprev) + (frac - fprev);
        if (MODE == 0 || cp <= 2) {
          Wl = (int)rintf(fminf(fmaxf(W0f - frac * (1.f / (MODE ? 40 : (KP ? KP : 1))), (float)WLO), (float)WHI));
          rctl[0] = Wl;
          lagf = -frac;
        } else {
          tau = fminf(iv, iv1) * ((1.f - 0.001f * TB) / CPI);
          tref = town;
          sch = frac + tau * (RDL + WDL + 1);
          on = true;
        }
        iv1 = iv; tprev = town; fprev = frac;
        if (DBG & 1) rlog = lane == cp ? (((unsigned)Wl << 16) | ((unsigned)(int)(-frac) & 0xffffu)) : rlog;
      }
#pragma unroll 1
      for (int i = 0; i < CPI - RDL - WDL; ++i) step();
    }
    bar(); bar(); bar();
    if ((DBG & 1) && stamps) stamps[4096 + bh * 64 + lane] = ((u64)rlog << 32) | tlog;
  } else {
    typedef __attribute__((ext_vector_type(LPW))) float fvec;
    const int w = wave - 1, c = lane & 15, r = lane >> 4;
    const u32x4 qp = *reinterpret_cast<const u32x4*>(q + bh * D + 8 * c);
    LAS fvec* sc = (LAS fvec*)(lds + SCB) + w * 512 + r;
    const LAS u32x4* ring = (const LAS u32x4*)lds + w * (LPW * 64) + lane;
    float m = -INFINITY;
    u32x4 x[LPW];
#pragma unroll 1
    for (int j = 0; j < 128; ++j) {
      lds_bar();
#pragma unroll
      for (int i = 0; i < LPW; ++i) x[i] = ring[(j & 3) * 1024 + i * 64];
      fvec s;
#pragma unroll
      for (int i = 0; i < LPW; ++i) {
        float d = dot2(x[i][0], qp[0], 0.f);
        d = dot2(x[i][1], qp[1], d); d = dot2(x[i][2], qp[2], d); d = dot2(x[i][3], qp[3], d);
        s[i] = row16_sum(d);
        m = fmaxf(m, s[i]);
      }
      if (c == 0) sc[j * 4] = s;
    }
    ((LAS float*)(lds + MXB))[w] = xrow_max(m);
    f32x2 acc[4];
#pragma unroll
    for (int i = 0; i < 4; ++i) acc[i] = f32x2{0.f, 0.f};
    float inv = 0.f;
#pragma unroll 1
    for (int j = 0; j < 128; ++j) {
      lds_bar();
#pragma unroll
      for (int i = 0; i < LPW; ++i) x[i] = ring[(j & 3) * 1024 + i * 64];
      if (j == 0) {
        float M = -INFINITY;
#pragma unroll
        for (int y = 0; y < NC; y += 4) { const f32x4 mx = *(const LAS f32x4*)(lds + MXB + y * 4); M = fmaxf(M, fmaxf(fmaxf(mx[0], mx[1]), fmaxf(mx[2], mx[3]))); }
        const float mc = -M * C2;
        LAS f32x4* e = (LAS f32x4*)(lds + SCB) + w * (LPW * 128) + lane;
        float l = 0.f;
#pragma unroll
        for (int y = 0; y < LPW * 2; ++y) {
          f32x4 t = e[y * 64];
#pragma unroll
          for (int i = 0; i < 4; ++i) t[i] = __builtin_amdgcn_exp2f(fmaf(t[i], C2, mc));
          l += (t[0] + t[1]) + (t[2] + t[3]);
          e[y * 64] = t;
        }
        l = row16_sum(l); l = swap32_add(l, l); l = swap16_add(l, l);
        ((LAS float*)(lds + LSB))[w] = l;
      } else if (j == 1) {
        float L = 0.f;
#pragma unroll
        for (int y = 0; y < NC; y += 4) { const f32x4 ls = *(const LAS f32x4*)(lds + LSB + y * 4); L += (ls[0] + ls[1]) + (ls[2] + ls[3]); }
        inv = 1.f / L;
      }
      const fvec p = sc[j * 4];
#pragma unroll
      for (int i = 0; i < LPW; ++i)
#pragma unroll
        for (int e = 0; e < 4; ++e) {
          const f32x2 vv = {__builtin_bit_cast(float, x[i][e] << 16), __builtin_bit_cast(float, x[i][e] & 0xffff0000u)};
          acc[e] = __builtin_elementwise_fma(f32x2{p[i], p[i]}, vv, acc[e]);
        }
    }
    const float u0 = swap32_add(acc[0][0], acc[2][0]), u1 = swap32_add(acc[0][1], acc[2][1]);
    const float u2 = swap32_add(acc[1][0], acc[3][0]), u3 = swap32_add(acc[1][1], acc[3][1]);
    const f32x2 o = {swap16_add(u0, u2), swap16_add(u1, u3)};
    LAS f32x2* red = (LAS f32x2*)(lds + REDB) + lane;
    red[w * 64] = o;
    lds_bar();
    if (w == 0) {
      f32x2 f = (o + red[64]) + (red[128] + red[192]);
      if (NC == 8) f += (red[256] + red[320]) + (red[384] + red[448]);
      f *= inv;
      const bf16x2 ob = __builtin_convertvector(f, bf16x2);
      unsigned* op = reinterpret_cast<unsigned*>(out + bh * D) + 4 * c + r;
      const unsigned ov = __builtin_bit_cast(unsigned, ob);
      if (ST == 0) *op = ov;
      else if (ST == 1) asm volatile("global_store_dword %0, %1, off nt" :: "v"(op), "v"(ov) : "memory");
      else if (ST == 2) asm volatile("global_store_dword %0, %1, off sc1" :: "v"(op), "v"(ov) : "memory");
      else asm volatile("global_store_dword %0, %1, off sc0 sc1" :: "v"(op), "v"(ov) : "memory");
      if ((DBG & 1) && stamps && lane == 0) {
        u64 t1 = __builtin_amdgcn_s_memrealtime();
        asm volatile("s_waitcnt vmcnt(0)" ::: "memory");
        u64 t2 = __builtin_amdgcn_s_memrealtime();
        unsigned xcc; asm volatile("s_getreg_b32 %0, hwreg(HW_REG_XCC_ID)" : "=s"(xcc));
        stamps[bh * 4] = t0; stamps[bh * 4 + 1] = t1; stamps[bh * 4 + 2] = xcc; stamps[bh * 4 + 3] = t2;
      }
    }
  }
}
}

extern "C" void kernel_launch(void* const* d_in, const int* in_sizes, int n_in, void* d_out, int out_size, void* d_ws,
                              size_t ws_size, hipStream_t stream) {
  const int BH = in_sizes[0] / v12::D;
  v12::attn<4,180,12,28,32,40,4,3,8,0,0,2,0,28,0><<<BH, 384, 0, stream>>>((const uint16_t*)d_in[0], (const uint16_t*)d_in[1], (const uint16_t*)d_in[2], (uint16_t*)d_out, (unsigned long long*)d_ws, nullptr);
}
```

```cpp
#include <hip/hip_runtime.h>
#include <stdint.h>

namespace {
typedef unsigned long long u64;
using u32x4 = __attribute__((ext_vector_type(4))) unsigned;
using f32x4 = __attribute__((ext_vector_type(4))) float;
using f32x2 = __attribute__((ext_vector_type(2))) float;
using bf16x2 = __attribute__((ext_vector_type(2))) __bf16;
using bf16x4 = __attribute__((ext_vector_type(4))) __bf16;
#define LAS __attribute__((address_space(3)))
typedef __attribute__((address_space(1))) unsigned gu32;
typedef __attribute__((address_space(1))) unsigned long long gu64;
typedef __attribute__((address_space(1))) u32x4 gu128;

constexpr int SK = 8192, D = 128, NHEAD = 256;
constexpr unsigned HEADB = SK * D * 2;
constexpr int NG = 256, GB = 16384, RINGB = 4 * GB;
constexpr int SOLO = 16, SEG = (128 - SOLO) / 4;
constexpr int PRE = 6;
constexpr int GAP = 3;
constexpr int NCP = 32, CPI = NG / NCP, NPUB = 8, RDL = 4, WDL = 3, I0 = 5;
constexpr float I0F = 4.8f, KP = 25.f, STALL = 150.f;
constexpr int SCW = 144 * 64;
constexpr int SCB = RINGB, REDB = SCB + 4 * SCW, QB = REDB + 4 * 512, MXB = QB + 1024, LSB = MXB + 80, RCB = LSB + 80,
              LDSB = RCB + 16;
constexpr int CST = 512 + 32, TWO = NCP * CST, SLO = TWO + 32 * 32;
constexpr size_t WS_NEED = (size_t)SLO * 8 + (size_t)768 * 640;
constexpr float C2 = 0.088388347648318440f * 1.4426950408889634f;
constexpr int SPIN_CAP = 1 << 21;
static_assert(SEG % 2 == 0 && 4 * SEG + SOLO == 128 && 4 * (32 - SEG) + 16 - SOLO == 16 && 4 * GAP + 1 < SOLO &&
              RDL + WDL <= CPI && LDSB <= 160 * 1024, "layout");

__device__ __forceinline__ float dot2(unsigned a, unsigned b, float acc) {
  return __builtin_amdgcn_fdot2_f32_bf16(__builtin_bit_cast(bf16x2, a), __builtin_bit_cast(bf16x2, b), acc, false);
}
template <int CTRL> __device__ __forceinline__ float dpp(float x) {
  return __builtin_bit_cast(float, __builtin_amdgcn_mov_dpp(__builtin_bit_cast(int, x), CTRL, 0xf, 0xf, true));
}
__device__ __forceinline__ float row16_sum(float x) {
  x += dpp<0xB1>(x); x += dpp<0x4E>(x); x += dpp<0x141>(x); x += dpp<0x128>(x); return x;
}
__device__ __forceinline__ float swap32_add(float a, float b) {
  auto t = __builtin_amdgcn_permlane32_swap(__float_as_uint(a), __float_as_uint(b), false, false);
  return __uint_as_float(t[0]) + __uint_as_float(t[1]);
}
__device__ __forceinline__ float swap16_add(float a, float b) {
  auto t = __builtin_amdgcn_permlane16_swap(__float_as_uint(a), __float_as_uint(b), false, false);
  return __uint_as_float(t[0]) + __uint_as_float(t[1]);
}
__device__ __forceinline__ float xrow_max(float x) {
  auto s = __builtin_amdgcn_permlane16_swap(__float_as_uint(x), __float_as_uint(x), false, false);
  x = fmaxf(__uint_as_float(s[0]), __uint_as_float(s[1]));
  auto t = __builtin_amdgcn_permlane32_swap(__float_as_uint(x), __float_as_uint(x), false, false);
  return fmaxf(__uint_as_float(t[0]), __uint_as_float(t[1]));
}
__device__ __forceinline__ float max4(f32x4 a) { return fmaxf(fmaxf(a[0], a[1]), fmaxf(a[2], a[3])); }
__device__ __forceinline__ float sum4(f32x4 a) { return (a[0] + a[1]) + (a[2] + a[3]); }
template <int W> __device__ __forceinline__ void waitvm() { asm volatile("s_waitcnt vmcnt(%0)" ::"n"(W) : "memory"); }
__device__ __forceinline__ void bar() { asm volatile("s_barrier" ::: "memory"); }
__device__ __forceinline__ void lds_bar() { asm volatile("s_waitcnt lgkmcnt(0)\n\ts_barrier" ::: "memory"); }
__device__ __forceinline__ void bars(int n) {
#pragma unroll 1
  for (; n > 0; --n) bar();
}
__device__ __forceinline__ void st128(gu128* p, u32x4 x) {
  asm volatile("global_store_dwordx4 %0, %1, off sc1 nt\n\ts_nop 1" ::"v"(p), "v"(x) : "memory");
}

#define TILE                                                              \
  "buffer_load_dwordx4 %[vo], s[60:63], s64 offen nt lds\n\t"             \
  "buffer_load_dwordx4 %[vo], s[60:63], s64 offen offset:1024 nt lds\n\t" \
  "buffer_load_dwordx4 %[vo], s[60:63], s64 offen offset:2048 nt lds\n\t" \
  "buffer_load_dwordx4 %[vo], s[60:63], s64 offen offset:3072 nt lds\n\t" \
  "s_add_u32 s64, s64, s72\n\ts_add_u32 m0, m0, s72\n\ts_cmp_lg_u32 s64, s65\n\t"
#define LEVEL(n) "2:\n\ts_waitcnt vmcnt(" #n ")\n\t" TILE "s_cbranch_scc1 2b\n\ts_branch 3f\n\t"
__device__ __forceinline__ void loader(int wave, u64 ka, u64 va, unsigned voff, unsigned ldsb, unsigned so0,
                                       unsigned sos, unsigned rc) {
  unsigned rn;
  asm volatile(
      "s_cmp_lg_u32 %[wv], 0\n\t"
      "s_cbranch_scc1 9f\n\t"
      "s_mov_b32 s60, %[kl]\n\ts_and_b32 s61, %[kh], 0xffff\n\ts_mov_b32 s62, %[nr]\n\ts_mov_b32 s63, 0x20000\n\t"
      "s_mov_b32 m0, %[lb]\n\ts_mov_b32 s64, %[so0]\n\ts_add_u32 s65, %[so0], %[pre]\n\ts_movk_i32 s72, 0x1000\n"
      "2:\n\t" TILE "s_cbranch_scc1 2b\n\t"
      "s_setprio 3\n\t"
      "s_add_u32 s65, %[so0], 0x8000\n\ts_movk_i32 s66, %[off0]\n\ts_mov_b32 s67, 0x18000\n\ts_mov_b32 s68, 0x18000\n\t"
      "s_movk_i32 s69, %[seg1]\n\ts_mov_b32 s70, 0\n\ts_mov_b32 s71, 1\n\ts_movk_i32 s73, 0x4000\n"
      "1:\n\t"
      "ds_read_b32 %[rn], %[rc]\n\t"
      "s_getpc_b64 vcc\n\ts_add_u32 vcc_lo, vcc_lo, s66\n\ts_addc_u32 vcc_hi, vcc_hi, 0\n\ts_setpc_b64 vcc\n\t"
      LEVEL(13) LEVEL(14) LEVEL(15) LEVEL(16) LEVEL(17) LEVEL(18) LEVEL(19) LEVEL(20) LEVEL(21) LEVEL(22) LEVEL(23)
      LEVEL(24) LEVEL(28)
      "3:\n\t"
      "s_add_u32 s64, s64, s67\n\ts_xor_b32 s67, s67, s68\n\t"
      "s_add_u32 s73, s73, 0x4000\n\ts_and_b32 s73, s73, 0xffff\n\ts_add_u32 m0, s73, %[lb]\n\t"
      "s_waitcnt lgkmcnt(0)\n\t"
      "v_med3_i32 %[rn], %[rn], 0, 11\n\tv_mad_u32_u24 %[rn], %[rn], 56, 12\n\t"
      "s_sub_u32 s69, s69, 1\n\ts_cmp_lg_u32 s69, 0\n\ts_cbranch_scc1 5f\n\t"
      "s_add_u32 s70, s70, 1\n\ts_movk_i32 s69, %[seg]\n\ts_add_u32 s64, s64, %[nxt]\n\t"
      "s_cmp_lt_u32 s70, 4\n\ts_cbranch_scc1 5f\n\t"
      "s_mov_b32 s64, %[sos]\n\ts_mov_b32 s68, 0\n\ts_movk_i32 s69, %[solo]\n\t"
      "s_cmp_eq_u32 s70, 4\n\ts_cbranch_scc1 5f\n\t"
      "s_mov_b32 s64, %[so0]\n\ts_mov_b32 s68, 0x18000\n\ts_movk_i32 s69, %[seg]\n\ts_mov_b32 s70, 0\n\t"
      "s_mov_b32 s60, %[vl]\n\ts_and_b32 s61, %[vh], 0xffff\n"
      "5:\n\t"
      "s_add_u32 s65, s64, 0x4000\n\t"
      "s_add_u32 s71, s71, 1\n\t"
      "s_cmp_lt_u32 s71, 3\n\ts_cbranch_scc1 1b\n\t"
      "s_barrier\n\t"
      "s_cmp_eq_u32 s71, 3\n\ts_cbranch_scc1 1b\n\t"
      "v_readfirstlane_b32 s66, %[rn]\n\t"
      "s_cmpk_lt_u32 s71, 255\n\ts_cbranch_scc1 1b\n\t"
      "s_movk_i32 s66, 684\n\t"
      "s_cmpk_eq_u32 s71, 255\n\ts_cbranch_scc1 1b\n\t"
      "s_waitcnt vmcnt(16)\n\ts_barrier\n\ts_waitcnt vmcnt(0)\n\ts_barrier\n\ts_barrier\n\ts_endpgm\n"
      "9:"
      : [rn] "=&v"(rn)
      : [wv] "s"(wave), [kl] "s"((unsigned)ka), [kh] "s"((unsigned)(ka >> 32)), [vl] "s"((unsigned)va),
        [vh] "s"((unsigned)(va >> 32)), [vo] "v"(voff), [lb] "s"(ldsb), [so0] "s"(so0), [sos] "s"(sos), [rc] "v"(rc),
        [nr] "n"(7 * HEADB), [pre] "n"(PRE * 4096), [off0] "n"(12 + 56 * I0), [seg1] "n"(SEG - 1), [seg] "n"(SEG),
        [solo] "n"(SOLO), [nxt] "n"(2 * HEADB - 4 * SEG * GB)
      : "vcc", "scc", "memory", "s60", "s61", "s62", "s63", "s64", "s65", "s66", "s67", "s68", "s69", "s70", "s71",
        "s72", "s73");
}

__global__ void __launch_bounds__(448, 2)
attn_decode(const uint16_t* __restrict__ q, const uint16_t* __restrict__ k, const uint16_t* __restrict__ v,
            uint16_t* __restrict__ out, u64* __restrict__ ws) {
  __shared__ __attribute__((aligned(16))) char lds_[LDSB];
  LAS char* lds = (LAS char*)lds_;
  volatile LAS int* rctl = (volatile LAS int*)(lds + RCB);
  const int bh = blockIdx.x;
  const int mem = (bh >> 1) & 3, h0 = bh & ~6;
  const int tid = threadIdx.x, lane = tid & 63, wave = __builtin_amdgcn_readfirstlane(tid >> 6);

  loader(wave, (u64)k + (size_t)h0 * HEADB, (u64)v + (size_t)h0 * HEADB, lane * 16, (unsigned)(size_t)lds,
         mem * 2 * GB, mem * 2 * HEADB + 4 * SEG * GB, (unsigned)(size_t)(lds + RCB));

  if (wave == 6) {
    gu32* tw = (gu32*)(ws + TWO + (bh >> 3) * 32);
    const int stg = (bh >> 3) & 15;
    bars(8 + stg);
    unsigned x0 = 0, wv = 0;
    const unsigned one = 1, ack = 256;
    if (lane == 0) asm volatile("global_atomic_add %0, %1, %2, off sc0" : "=&v"(x0) : "v"(tw), "v"(one) : "memory");
    bars(24);
    asm volatile("s_waitcnt vmcnt(0)" : "+v"(x0)::"memory");
    const unsigned xs = __builtin_amdgcn_readfirstlane(x0);
    unsigned tag = xs;
    bool acked = false, bad = false;
#pragma unroll 1
    for (int it = 0; tag - xs < 256; ++it) {
      asm volatile("global_load_dword %0, %1, off sc1" : "=v"(wv) : "v"(tw) : "memory");
      if (it < 2) bars(4);
      asm volatile("s_waitcnt vmcnt(0)" : "+v"(wv)::"memory");
      tag = __builtin_amdgcn_readfirstlane(wv);
      if (tag - xs == 8 && !acked) {
        acked = true;
        if (lane == 0) asm volatile("global_atomic_add %0, %1, off" ::"v"(tw), "v"(ack) : "memory");
      }
      if (it == 0) bars(tag - xs < 256 ? 28 : 32);
      else if (it > 1) __builtin_amdgcn_s_sleep(8);
      if (it > SPIN_CAP) { bad = true; break; }
    }
    int nb = 68 + stg;
    const int L = lane & 31;
    const LAS f32x4* red = (const LAS f32x4*)(lds + REDB) + L;
    const LAS f32x4* mx = (const LAS f32x4*)(lds + MXB);
    const LAS f32x4* ls = (const LAS f32x4*)(lds + LSB);
    gu128* sl0 = (gu128*)(ws + SLO) + ((bh >> 3) * 2 + (bh & 1)) * 120;
    f32x4 oown = {0.f, 0.f, 0.f, 0.f};
    u32x4 a0 = {0, 0, 0, 0}, a1 = a0, a2 = a0, b0 = a0, b1 = a0, b2 = a0;
#define LOAD3(x0, x1, x2, p)                                                                                     \
  asm volatile("global_load_dwordx4 %0, %3, off sc1 nt\n\tglobal_load_dwordx4 %1, %3, off offset:640 sc1 nt\n\t" \
               "global_load_dwordx4 %2, %3, off offset:1280 sc1 nt"                                              \
               : "=&v"(x0), "=&v"(x1), "=&v"(x2) : "v"(p) : "memory")
#pragma unroll 1
    for (int u = 0; u < 4; ++u) {
      const int E = 128 + SEG * (u + 1) + 1;
      bars(E - nb);
      f32x4 o = (red[0] + red[32]) + (red[64] + red[96]);
      u32x4 fl = {__float_as_uint(C2 * max4(mx[u])), tag, __float_as_uint(sum4(ls[u])), tag};
      asm volatile("s_waitcnt lgkmcnt(0)" : "+v"(o), "+v"(fl)::"memory");
      gu128* sl = sl0 + u * (64 * 120);
      if (u != mem) {
        sl += ((mem - u - 1) & 3) * 40;
        if (lane < 32) st128(sl + lane, __builtin_bit_cast(u32x4, o));
        bars(GAP);
        waitvm<0>();
        if (lane < 8 && !bad) st128(sl + 32 + lane, fl);
        nb = E + GAP;
      } else {
        oown = o;
        bars(2 * GAP);
#pragma unroll 1
        for (int it = 0;; ++it) {
          LOAD3(b0, b1, b2, sl + 32);
          if (it == 0) bars(GAP);
          asm volatile("s_waitcnt vmcnt(0)" : "+v"(b0), "+v"(b1), "+v"(b2)::"memory");
          const bool ok = b0[1] == tag && b0[3] == tag && b1[1] == tag && b1[3] == tag && b2[1] == tag && b2[3] == tag;
          if (__all(ok)) break;
          if (bad || it > SPIN_CAP) { bad = true; break; }
          __builtin_amdgcn_s_sleep(4);
        }
        LOAD3(a0, a1, a2, sl + L);
        bars(GAP);
        asm volatile("s_waitcnt vmcnt(0)" : "+v"(a0), "+v"(a1), "+v"(a2)::"memory");
        nb = E + 4 * GAP;
      }
    }
#undef LOAD3
    float mo = C2 * fmaxf(max4(mx[mem]), max4(mx[4]));
    asm volatile("" : "+v"(mo));
    const float lo = sum4(ls[mem]) + sum4(ls[4]);
    const float m0 = __uint_as_float(b0[0]), m1 = __uint_as_float(b1[0]), m2 = __uint_as_float(b2[0]);
    const float M = fmaxf(fmaxf(mo, m0), fmaxf(m1, m2));
    const float wo = __builtin_amdgcn_exp2f(mo - M), w0 = __builtin_amdgcn_exp2f(m0 - M);
    const float w1 = __builtin_amdgcn_exp2f(m1 - M), w2 = __builtin_amdgcn_exp2f(m2 - M);
    float inv = 1.f / (fmaf(wo, lo, w0 * __uint_as_float(b0[2])) + fmaf(w1, __uint_as_float(b1[2]), w2 * __uint_as_float(b2[2])));
    if (bad) inv = __builtin_nanf("");
    f32x4 pre = (oown * wo + __builtin_bit_cast(f32x4, a0) * w0) +
                (__builtin_bit_cast(f32x4, a1) * w1 + __builtin_bit_cast(f32x4, a2) * w2);
    pre *= inv;
    float cs = wo * inv;
    gu64* op = (gu64*)(out + bh * D) + L;
    asm volatile("s_waitcnt lgkmcnt(0)" : "+v"(pre), "+v"(cs), "+v"(op)::"memory");
    bars(257 - nb);
    const f32x4 f = pre + ((red[0] + red[32]) + (red[64] + red[96])) * cs;
    if (lane < 32) *op = __builtin_bit_cast(u64, __builtin_convertvector(f, bf16x4));
  } else if (wave == 5) {
    rctl[0] = I0;
    gu64* cw = (gu64*)ws;
    u64 basev;
    asm volatile("global_load_dwordx2 %0, %1, off sc1" : "=v"(basev) : "v"(cw + (lane & (NCP - 1)) * CST) : "memory");
    unsigned tref = 0, tprev = 0, tnow = 0;
    float sch = 0.f, tau = 0.f, iv1 = 1e9f, fprev = 0.f, lagf = 0.f, lagp = 0.f, stl = 0.f;
    bool on = false;
    auto steps = [&](int n) {
#pragma unroll 1
      for (; n > 0; --n) {
        lds_bar();
        tnow = (unsigned)__builtin_amdgcn_s_memrealtime();
        sch += tau;
        if (on) {
          float lag = (float)(int)(tnow - tref) - sch;
          if (lag - lagp > STALL) { const float jump = lag - lagp; sch += jump; stl += jump; lag = lagp; }
          lagp = lag;
          lagf = 0.5f * (lagf + lag);
          rctl[0] = (int)rintf(fminf(fmaxf(I0F + lagf * (1.f / KP), 0.f), 11.f));
        }
      }
    };
    steps(CPI - 2);
    asm volatile("s_waitcnt vmcnt(0)" : "+v"(basev)::"memory");
#pragma unroll 1
    for (int cp = 1; cp < NCP; ++cp) {
      const unsigned town = tnow;
      stl = 0.f;
      const u64 addv = (1ull << 40) + town;
      if (bh < NPUB && lane == 0)
        asm volatile("global_atomic_add_x2 %0, %1, off" ::"v"(cw + cp * CST), "v"(addv) : "memory");
      steps(RDL);
      const unsigned trd = tnow;
      u64 rdv;
      asm volatile("global_load_dwordx2 %0, %1, off sc1" : "=v"(rdv) : "v"(cw + cp * CST) : "memory");
      steps(WDL);
      asm volatile("s_waitcnt vmcnt(0)" : "+v"(rdv)::"memory");
      const unsigned blo = __builtin_amdgcn_readlane((unsigned)basev, cp);
      const unsigned bhi = __builtin_amdgcn_readlane((unsigned)(basev >> 32), cp);
      const u64 d = rdv - (((u64)bhi << 32) | blo);
      const unsigned cnt = min((unsigned)(d >> 40), (unsigned)NPUB);
      const int rel = (int)((unsigned)d - cnt * town) + (int)((NPUB - cnt) * (trd - town));
      const float frac = (float)rel * (1.f / NPUB);
      const float iv = (float)(int)(town - tprev) + (frac - fprev);
      if (cp <= 2) {
        rctl[0] = (int)rintf(fminf(fmaxf(I0F - frac * (1.f / 40.f), 0.f), 11.f));
      } else {
        const float cur = sch + (float)(int)(tref - town);
        tau = fminf(iv, iv1) * (0.998f / CPI);
        float nw = frac + stl + tau * (RDL + WDL);
        if (on) nw = cur + fminf(fmaxf(nw - cur, -40.f), 40.f);
        else lagf = -frac;
        tref = town;
        sch = nw;
        lagp = (float)(int)(tnow - town) - nw;
        on = true;
      }
      iv1 = iv; tprev = town; fprev = frac;
      steps(CPI - RDL - WDL);
    }
    bars(3);
  } else {
    const int w = wave - 1, c = lane & 15, r = lane >> 4;
    u32x4 qp = *reinterpret_cast<const u32x4*>(q + h0 * D + 8 * c);
    const unsigned qw = reinterpret_cast<const unsigned*>(q + (h0 + 2 * w) * D)[lane];
    LAS f32x4* sc = (LAS f32x4*)(lds + SCB + w * SCW);
    {
      constexpr int NU = 32 - SEG;
      const int i = lane >> 2;
      const int sl = i < 4 * NU ? (i / NU) * 32 + SEG + i % NU : 128 + SOLO + (i - 4 * NU);
      sc[sl * 4 + (lane & 3)] = f32x4{-INFINITY, -INFINITY, -INFINITY, -INFINITY};
    }
    const LAS u32x4* ring = (const LAS u32x4*)lds + w * 256 + lane;
    u32x4 x[4];
    int j = 0;
#pragma unroll 1
    for (int seg = 0; seg < 5; ++seg) {
      const int n = seg < 4 ? SEG : SOLO;
      LAS f32x4* sp = sc + seg * 128 + r;
      float m = -INFINITY;
#pragma unroll 1
      for (int i0 = 0; i0 < n; ++i0, ++j) {
        lds_bar();
#pragma unroll
        for (int i = 0; i < 4; ++i) x[i] = ring[(j & 3) * 1024 + i * 64];
        f32x4 s;
#pragma unroll
        for (int i = 0; i < 4; ++i) {
          float d = dot2(x[i][0], qp[0], 0.f);
          d = dot2(x[i][1], qp[1], d); d = dot2(x[i][2], qp[2], d); d = dot2(x[i][3], qp[3], d);
          s[i] = row16_sum(d);
          m = fmaxf(m, s[i]);
        }
        if (c == 0) sp[i0 * 4] = s;
        if (j == 0) ((LAS unsigned*)(lds + QB))[w * 64 + lane] = qw;
      }
      ((LAS float*)(lds + MXB))[seg * 4 + w] = xrow_max(m);
      qp = ((const LAS u32x4*)(lds + QB))[(seg < 3 ? seg + 1 : mem) * 16 + c];
    }
#pragma unroll 1
    for (int seg = 0; seg < 5; ++seg) {
      const int n = seg < 4 ? SEG : SOLO;
      const LAS f32x4* sp = sc + seg * 128 + r;
      f32x2 acc[4];
#pragma unroll
      for (int i = 0; i < 4; ++i) acc[i] = f32x2{0.f, 0.f};
#pragma unroll 1
      for (int i0 = 0; i0 < n; ++i0, ++j) {
        lds_bar();
#pragma unroll
        for (int i = 0; i < 4; ++i) x[i] = ring[(j & 3) * 1024 + i * 64];
        if (i0 == 0) {
          const bool own = seg == mem || seg == 4;
          const f32x4 ma = ((const LAS f32x4*)(lds + MXB))[own ? mem : seg];
          const f32x4 mb = ((const LAS f32x4*)(lds + MXB))[own ? 4 : seg];
          const float mc = -(C2 * fmaxf(max4(ma), max4(mb)));
          LAS f32x4* e = sc + seg * 128 + lane;
          float l = 0.f;
#pragma unroll 1
          for (int y = 0; y < (seg < 4 ? 2 : 1); ++y) {
            f32x4 t = e[y * 64];
#pragma unroll
            for (int i = 0; i < 4; ++i) t[i] = __builtin_amdgcn_exp2f(fmaf(t[i], C2, mc));
            l += sum4(t);
            e[y * 64] = t;
          }
          l = row16_sum(l); l = swap32_add(l, l); l = swap16_add(l, l);
          ((LAS float*)(lds + LSB))[seg * 4 + w] = l;
        }
        const f32x4 p = sp[i0 * 4];
#pragma unroll
        for (int i = 0; i < 4; ++i)
#pragma unroll
          for (int e = 0; e < 4; ++e) {
            const f32x2 vv = {__builtin_bit_cast(float, x[i][e] << 16), __builtin_bit_cast(float, x[i][e] & 0xffff0000u)};
            acc[e] = __builtin_elementwise_fma(f32x2{p[i], p[i]}, vv, acc[e]);
          }
      }
      const float u0 = swap32_add(acc[0][0], acc[2][0]), u1 = swap32_add(acc[0][1], acc[2][1]);
      const float u2 = swap32_add(acc[1][0], acc[3][0]), u3 = swap32_add(acc[1][1], acc[3][1]);
      ((LAS f32x2*)(lds + REDB))[w * 64 + 4 * c + r] = f32x2{swap16_add(u0, u2), swap16_add(u1, u3)};
    }
    lds_bar();
  }
}
}

extern "C" void kernel_launch(void* const* d_in, const int* in_sizes, int n_in, void* d_out, int out_size, void* d_ws,
                              size_t ws_size, hipStream_t stream) {
  if (n_in != 3 || in_sizes[0] != NHEAD * D || out_size != NHEAD * D || ws_size < WS_NEED) return;
  attn_decode<<<NHEAD, 448, 0, stream>>>((const uint16_t*)d_in[0], (const uint16_t*)d_in[1], (const uint16_t*)d_in[2],
                                         (uint16_t*)d_out, (u64*)d_ws);
}
```

```cpp
#include <hip/hip_runtime.h>
#include <stdint.h>

namespace {
typedef unsigned long long u64;
using u32x4 = __attribute__((ext_vector_type(4))) unsigned;
using f32x4 = __attribute__((ext_vector_type(4))) float;
using f32x2 = __attribute__((ext_vector_type(2))) float;
using bf16x2 = __attribute__((ext_vector_type(2))) __bf16;
using bf16x4 = __attribute__((ext_vector_type(4))) __bf16;
#define LAS __attribute__((address_space(3)))
typedef __attribute__((address_space(1))) unsigned gu32;
typedef __attribute__((address_space(1))) unsigned long long gu64;
typedef __attribute__((address_space(1))) u32x4 gu128;

constexpr int SK = 8192, D = 128, NHEAD = 256;
constexpr unsigned HEADB = SK * D * 2;
constexpr int NG = 256, GB = 16384, RINGB = 4 * GB;
constexpr int SOLO = 16, SEG = (128 - SOLO) / 4;
constexpr int PRE = 6;
constexpr int GAP = 3;
constexpr int NCP = 32, CPI = NG / NCP, NPUB = 8, RDL = 4, WDL = 3, I0 = 5;
constexpr float I0F = 4.5f, KP = 25.f, STALL = 150.f;
constexpr int SCW = 144 * 64;
constexpr int SCB = RINGB, REDB = SCB + 4 * SCW, QB = REDB + 4 * 512, MXB = QB + 1024, LSB = MXB + 80, RCB = LSB + 80,
              LDSB = RCB + 16;
constexpr int CST = 512 + 32, TWO = NCP * CST, SLO = TWO + 32 * 32;
constexpr size_t WS_NEED = (size_t)SLO * 8 + (size_t)768 * 640;
constexpr float C2 = 0.088388347648318440f * 1.4426950408889634f;
constexpr int SPIN_CAP = 1 << 21;
static_assert(SEG % 2 == 0 && 4 * SEG + SOLO == 128 && 4 * (32 - SEG) + 16 - SOLO == 16 && 4 * GAP + 1 < SOLO &&
              RDL + WDL <= CPI && LDSB <= 160 * 1024, "layout");

__device__ __forceinline__ float dot2(unsigned a, unsigned b, float acc) {
  return __builtin_amdgcn_fdot2_f32_bf16(__builtin_bit_cast(bf16x2, a), __builtin_bit_cast(bf16x2, b), acc, false);
}
template <int CTRL> __device__ __forceinline__ float dpp(float x) {
  return __builtin_bit_cast(float, __builtin_amdgcn_mov_dpp(__builtin_bit_cast(int, x), CTRL, 0xf, 0xf, true));
}
__device__ __forceinline__ float row16_sum(float x) {
  x += dpp<0xB1>(x); x += dpp<0x4E>(x); x += dpp<0x141>(x); x += dpp<0x128>(x); return x;
}
__device__ __forceinline__ float swap32_add(float a, float b) {
  auto t = __builtin_amdgcn_permlane32_swap(__float_as_uint(a), __float_as_uint(b), false, false);
  return __uint_as_float(t[0]) + __uint_as_float(t[1]);
}
__device__ __forceinline__ float swap16_add(float a, float b) {
  auto t = __builtin_amdgcn_permlane16_swap(__float_as_uint(a), __float_as_uint(b), false, false);
  return __uint_as_float(t[0]) + __uint_as_float(t[1]);
}
__device__ __forceinline__ float xrow_max(float x) {
  auto s = __builtin_amdgcn_permlane16_swap(__float_as_uint(x), __float_as_uint(x), false, false);
  x = fmaxf(__uint_as_float(s[0]), __uint_as_float(s[1]));
  auto t = __builtin_amdgcn_permlane32_swap(__float_as_uint(x), __float_as_uint(x), false, false);
  return fmaxf(__uint_as_float(t[0]), __uint_as_float(t[1]));
}
__device__ __forceinline__ float max4(f32x4 a) { return fmaxf(fmaxf(a[0], a[1]), fmaxf(a[2], a[3])); }
__device__ __forceinline__ float sum4(f32x4 a) { return (a[0] + a[1]) + (a[2] + a[3]); }
template <int W> __device__ __forceinline__ void waitvm() { asm volatile("s_waitcnt vmcnt(%0)" ::"n"(W) : "memory"); }
__device__ __forceinline__ void bar() { asm volatile("s_barrier" ::: "memory"); }
__device__ __forceinline__ void lds_bar() { asm volatile("s_waitcnt lgkmcnt(0)\n\ts_barrier" ::: "memory"); }
__device__ __forceinline__ void bars(int n) {
#pragma unroll 1
  for (; n > 0; --n) bar();
}
__device__ __forceinline__ void st128(gu128* p, u32x4 x) {
  asm volatile("global_store_dwordx4 %0, %1, off sc1\n\ts_nop 1" ::"v"(p), "v"(x) : "memory");
}

#define TILE                                                              \
  "buffer_load_dwordx4 %[vo], s[60:63], s64 offen nt lds\n\t"             \
  "buffer_load_dwordx4 %[vo], s[60:63], s64 offen offset:1024 nt lds\n\t" \
  "buffer_load_dwordx4 %[vo], s[60:63], s64 offen offset:2048 nt lds\n\t" \
  "buffer_load_dwordx4 %[vo], s[60:63], s64 offen offset:3072 nt lds\n\t" \
  "s_add_u32 s64, s64, s72\n\ts_add_u32 m0, m0, s72\n\ts_cmp_lg_u32 s64, s65\n\t"
#define LEVEL(n) "2:\n\ts_waitcnt vmcnt(" #n ")\n\t" TILE "s_cbranch_scc1 2b\n\ts_branch 3f\n\t"
__device__ __forceinline__ void loader(int wave, u64 ka, u64 va, unsigned voff, unsigned ldsb, unsigned so0,
                                       unsigned sos, unsigned rc) {
  unsigned rn;
  asm volatile(
      "s_cmp_lg_u32 %[wv], 0\n\t"
      "s_cbranch_scc1 9f\n\t"
      "s_mov_b32 s60, %[kl]\n\ts_and_b32 s61, %[kh], 0xffff\n\ts_mov_b32 s62, %[nr]\n\ts_mov_b32 s63, 0x20000\n\t"
      "s_mov_b32 m0, %[lb]\n\ts_mov_b32 s64, %[so0]\n\ts_add_u32 s65, %[so0], %[pre]\n\ts_movk_i32 s72, 0x1000\n"
      "2:\n\t" TILE "s_cbranch_scc1 2b\n\t"
      "s_setprio 3\n\t"
      "s_add_u32 s65, %[so0], 0x8000\n\ts_movk_i32 s66, %[off0]\n\ts_mov_b32 s67, 0x18000\n\ts_mov_b32 s68, 0x18000\n\t"
      "s_movk_i32 s69, %[seg1]\n\ts_mov_b32 s70, 0\n\ts_mov_b32 s71, 1\n\ts_movk_i32 s73, 0x4000\n"
      "1:\n\t"
      "ds_read_b32 %[rn], %[rc]\n\t"
      "s_getpc_b64 vcc\n\ts_add_u32 vcc_lo, vcc_lo, s66\n\ts_addc_u32 vcc_hi, vcc_hi, 0\n\ts_setpc_b64 vcc\n\t"
      LEVEL(13) LEVEL(14) LEVEL(15) LEVEL(16) LEVEL(17) LEVEL(18) LEVEL(19) LEVEL(20) LEVEL(21) LEVEL(22) LEVEL(23)
      LEVEL(24) LEVEL(28)
      "3:\n\t"
      "s_add_u32 s64, s64, s67\n\ts_xor_b32 s67, s67, s68\n\t"
      "s_add_u32 s73, s73, 0x4000\n\ts_and_b32 s73, s73, 0xffff\n\ts_add_u32 m0, s73, %[lb]\n\t"
      "s_waitcnt lgkmcnt(0)\n\t"
      "v_med3_i32 %[rn], %[rn], 0, 11\n\tv_mad_u32_u24 %[rn], %[rn], 56, 12\n\t"
      "s_sub_u32 s69, s69, 1\n\ts_cmp_lg_u32 s69, 0\n\ts_cbranch_scc1 5f\n\t"
      "s_add_u32 s70, s70, 1\n\ts_movk_i32 s69, %[seg]\n\ts_add_u32 s64, s64, %[nxt]\n\t"
      "s_cmp_lt_u32 s70, 4\n\ts_cbranch_scc1 5f\n\t"
      "s_mov_b32 s64, %[sos]\n\ts_mov_b32 s68, 0\n\ts_movk_i32 s69, %[solo]\n\t"
      "s_cmp_eq_u32 s70, 4\n\ts_cbranch_scc1 5f\n\t"
      "s_mov_b32 s64, %[so0]\n\ts_mov_b32 s68, 0x18000\n\ts_movk_i32 s69, %[seg]\n\ts_mov_b32 s70, 0\n\t"
      "s_mov_b32 s60, %[vl]\n\ts_and_b32 s61, %[vh], 0xffff\n"
      "5:\n\t"
      "s_add_u32 s65, s64, 0x4000\n\t"
      "s_add_u32 s71, s71, 1\n\t"
      "s_cmp_lt_u32 s71, 3\n\ts_cbranch_scc1 1b\n\t"
      "s_barrier\n\t"
      "s_cmp_eq_u32 s71, 3\n\ts_cbranch_scc1 1b\n\t"
      "v_readfirstlane_b32 s66, %[rn]\n\t"
      "s_cmpk_lt_u32 s71, 255\n\ts_cbranch_scc1 1b\n\t"
      "s_movk_i32 s66, 684\n\t"
      "s_cmpk_eq_u32 s71, 255\n\ts_cbranch_scc1 1b\n\t"
      "s_waitcnt vmcnt(16)\n\ts_barrier\n\ts_waitcnt vmcnt(0)\n\ts_barrier\n\ts_barrier\n\ts_endpgm\n"
      "9:"
      : [rn] "=&v"(rn)
      : [wv] "s"(wave), [kl] "s"((unsigned)ka), [kh] "s"((unsigned)(ka >> 32)), [vl] "s"((unsigned)va),
        [vh] "s"((unsigned)(va >> 32)), [vo] "v"(voff), [lb] "s"(ldsb), [so0] "s"(so0), [sos] "s"(sos), [rc] "v"(rc),
        [nr] "n"(7 * HEADB), [pre] "n"(PRE * 4096), [off0] "n"(12 + 56 * I0), [seg1] "n"(SEG - 1), [seg] "n"(SEG),
        [solo] "n"(SOLO), [nxt] "n"(2 * HEADB - 4 * SEG * GB)
      : "vcc", "scc", "memory", "s60", "s61", "s62", "s63", "s64", "s65", "s66", "s67", "s68", "s69", "s70", "s71",
        "s72", "s73");
}

__global__ void __launch_bounds__(448, 2)
attn_decode(const uint16_t* __restrict__ q, const uint16_t* __restrict__ k, const uint16_t* __restrict__ v,
            uint16_t* __restrict__ out, u64* __restrict__ ws) {
  __shared__ __attribute__((aligned(16))) char lds_[LDSB];
  LAS char* lds = (LAS char*)lds_;
  volatile LAS int* rctl = (volatile LAS int*)(lds + RCB);
  const int bh = blockIdx.x;
  const int mem = (bh >> 1) & 3, h0 = bh & ~6;
  const int tid = threadIdx.x, lane = tid & 63, wave = __builtin_amdgcn_readfirstlane(tid >> 6);

  loader(wave, (u64)k + (size_t)h0 * HEADB, (u64)v + (size_t)h0 * HEADB, lane * 16, (unsigned)(size_t)lds,
         mem * 2 * GB, mem * 2 * HEADB + 4 * SEG * GB, (unsigned)(size_t)(lds + RCB));

  if (wave == 6) {
    gu32* tw = (gu32*)(ws + TWO + (bh >> 3) * 32);
    const int stg = (bh >> 3) & 15;
    bars(8 + stg);
    unsigned x0 = 0, wv = 0;
    const unsigned one = 1, ack = 256;
    if (lane == 0) asm volatile("global_atomic_add %0, %1, %2, off sc0" : "=&v"(x0) : "v"(tw), "v"(one) : "memory");
    bars(24);
    asm volatile("s_waitcnt vmcnt(0)" : "+v"(x0)::"memory");
    const unsigned xs = __builtin_amdgcn_readfirstlane(x0);
    unsigned tag = xs;
    bool acked = false, bad = false;
#pragma unroll 1
    for (int it = 0; tag - xs < 256; ++it) {
      asm volatile("global_load_dword %0, %1, off sc1" : "=v"(wv) : "v"(tw) : "memory");
      if (it < 2) bars(4);
      asm volatile("s_waitcnt vmcnt(0)" : "+v"(wv)::"memory");
      tag = __builtin_amdgcn_readfirstlane(wv);
      if (tag - xs == 8 && !acked) {
        acked = true;
        if (lane == 0) asm volatile("global_atomic_add %0, %1, off" ::"v"(tw), "v"(ack) : "memory");
      }
      if (it == 0) bars(tag - xs < 256 ? 28 : 32);
      else if (it > 1) __builtin_amdgcn_s_sleep(8);
      if (it > SPIN_CAP) { bad = true; break; }
    }
    int nb = 68 + stg;
    const int L = lane & 31;
    const LAS f32x4* red = (const LAS f32x4*)(lds + REDB) + L;
    const LAS f32x4* mx = (const LAS f32x4*)(lds + MXB);
    const LAS f32x4* ls = (const LAS f32x4*)(lds + LSB);
    gu128* sl0 = (gu128*)(ws + SLO) + ((bh >> 3) * 2 + (bh & 1)) * 120;
    f32x4 oown = {0.f, 0.f, 0.f, 0.f};
    u32x4 a0 = {0, 0, 0, 0}, a1 = a0, a2 = a0, b0 = a0, b1 = a0, b2 = a0;
#define LOAD3(x0, x1, x2, p)                                                                                     \
  asm volatile("global_load_dwordx4 %0, %3, off sc1\n\tglobal_load_dwordx4 %1, %3, off offset:640 sc1\n\t" \
               "global_load_dwordx4 %2, %3, off offset:1280 sc1"                                              \
               : "=&v"(x0), "=&v"(x1), "=&v"(x2) : "v"(p) : "memory")
#pragma unroll 1
    for (int u = 0; u < 4; ++u) {
      const int E = 128 + SEG * (u + 1) + 1;
      bars(E - nb);
      f32x4 o = (red[0] + red[32]) + (red[64] + red[96]);
      u32x4 fl = {__float_as_uint(C2 * max4(mx[u])), tag, __float_as_uint(sum4(ls[u])), tag};
      asm volatile("s_waitcnt lgkmcnt(0)" : "+v"(o), "+v"(fl)::"memory");
      gu128* sl = sl0 + u * (64 * 120);
      if (u != mem) {
        sl += ((mem - u - 1) & 3) * 40;
        if (lane < 32) st128(sl + lane, __builtin_bit_cast(u32x4, o));
        bars(GAP);
        waitvm<0>();
        if (lane < 8 && !bad) st128(sl + 32 + lane, fl);
        nb = E + GAP;
      } else {
        oown = o;
        bars(2 * GAP);
#pragma unroll 1
        for (int it = 0;; ++it) {
          LOAD3(b0, b1, b2, sl + 32);
          if (it == 0) bars(GAP);
          asm volatile("s_waitcnt vmcnt(0)" : "+v"(b0), "+v"(b1), "+v"(b2)::"memory");
          const bool ok = b0[1] == tag && b0[3] == tag && b1[1] == tag && b1[3] == tag && b2[1] == tag && b2[3] == tag;
          if (__all(ok)) break;
          if (bad || it > SPIN_CAP) { bad = true; break; }
          __builtin_amdgcn_s_sleep(4);
        }
        LOAD3(a0, a1, a2, sl + L);
        bars(GAP);
        asm volatile("s_waitcnt vmcnt(0)" : "+v"(a0), "+v"(a1), "+v"(a2)::"memory");
        nb = E + 4 * GAP;
      }
    }
#undef LOAD3
    float mo = C2 * fmaxf(max4(mx[mem]), max4(mx[4]));
    asm volatile("" : "+v"(mo));
    const float lo = sum4(ls[mem]) + sum4(ls[4]);
    const float m0 = __uint_as_float(b0[0]), m1 = __uint_as_float(b1[0]), m2 = __uint_as_float(b2[0]);
    const float M = fmaxf(fmaxf(mo, m0), fmaxf(m1, m2));
    const float wo = __builtin_amdgcn_exp2f(mo - M), w0 = __builtin_amdgcn_exp2f(m0 - M);
    const float w1 = __builtin_amdgcn_exp2f(m1 - M), w2 = __builtin_amdgcn_exp2f(m2 - M);
    float inv = 1.f / (fmaf(wo, lo, w0 * __uint_as_float(b0[2])) + fmaf(w1, __uint_as_float(b1[2]), w2 * __uint_as_float(b2[2])));
    if (bad) inv = __builtin_nanf("");
    f32x4 pre = (oown * wo + __builtin_bit_cast(f32x4, a0) * w0) +
                (__builtin_bit_cast(f32x4, a1) * w1 + __builtin_bit_cast(f32x4, a2) * w2);
    pre *= inv;
    float cs = wo * inv;
    gu64* op = (gu64*)(out + bh * D) + L;
    asm volatile("s_waitcnt lgkmcnt(0)" : "+v"(pre), "+v"(cs), "+v"(op)::"memory");
    bars(257 - nb);
    const f32x4 f = pre + ((red[0] + red[32]) + (red[64] + red[96])) * cs;
    if (lane < 32) *op = __builtin_bit_cast(u64, __builtin_convertvector(f, bf16x4));
  } else if (wave == 5) {
    rctl[0] = I0;
    gu64* cw = (gu64*)ws;
    u64 basev;
    asm volatile("global_load_dwordx2 %0, %1, off sc1" : "=v"(basev) : "v"(cw + (lane & (NCP - 1)) * CST) : "memory");
    unsigned tref = 0, tprev = 0, tnow = 0;
    float sch = 0.f, tau = 0.f, iv1 = 1e9f, fprev = 0.f, lagf = 0.f, lagp = 0.f, stl = 0.f;
    bool on = false;
    auto steps = [&](int n) {
#pragma unroll 1
      for (; n > 0; --n) {
        lds_bar();
        tnow = (unsigned)__builtin_amdgcn_s_memrealtime();
        sch += tau;
        if (on) {
          float lag = (float)(int)(tnow - tref) - sch;
          if (lag - lagp > STALL) { const float jump = lag - lagp; sch += jump; stl += jump; lag = lagp; }
          lagp = lag;
          lagf = 0.5f * (lagf + lag);
          rctl[0] = (int)rintf(fminf(fmaxf(I0F + lagf * (1.f / KP), 0.f), 11.f));
        }
      }
    };
    steps(CPI - 2);
    asm volatile("s_waitcnt vmcnt(0)" : "+v"(basev)::"memory");
#pragma unroll 1
    for (int cp = 1; cp < NCP; ++cp) {
      const unsigned town = tnow;
      stl = 0.f;
      const u64 addv = (1ull << 40) + town;
      if (bh < NPUB && lane == 0)
        asm volatile("global_atomic_add_x2 %0, %1, off" ::"v"(cw + cp * CST), "v"(addv) : "memory");
      steps(RDL);
      const unsigned trd = tnow;
      u64 rdv;
      asm volatile("global_load_dwordx2 %0, %1, off sc1" : "=v"(rdv) : "v"(cw + cp * CST) : "memory");
      steps(WDL);
      asm volatile("s_waitcnt vmcnt(0)" : "+v"(rdv)::"memory");
      const unsigned blo = __builtin_amdgcn_readlane((unsigned)basev, cp);
      const unsigned bhi = __builtin_amdgcn_readlane((unsigned)(basev >> 32), cp);
      const u64 d = rdv - (((u64)bhi << 32) | blo);
      const unsigned cnt = min((unsigned)(d >> 40), (unsigned)NPUB);
      const int rel = (int)((unsigned)d - cnt * town) + (int)((NPUB - cnt) * (trd - town));
      const float frac = (float)rel * (1.f / NPUB);
      const float iv = (float)(int)(town - tprev) + (frac - fprev);
      if (cp <= 2) {
        rctl[0] = (int)rintf(fminf(fmaxf(I0F - frac * (1.f / 40.f), 0.f), 11.f));
      } else {
        const float cur = sch + (float)(int)(tref - town);
        tau = fminf(iv, iv1) * (0.998f / CPI);
        float nw = frac + stl + tau * (RDL + WDL);
        if (on) nw = cur + fminf(fmaxf(nw - cur, -40.f), 40.f);
        else lagf = -frac;
        tref = town;
        sch = nw;
        lagp = (float)(int)(tnow - town) - nw;
        on = true;
      }
      iv1 = iv; tprev = town; fprev = frac;
      steps(CPI - RDL - WDL);
    }
    bars(3);
  } else {
    const int w = wave - 1, c = lane & 15, r = lane >> 4;
    u32x4 qp = *reinterpret_cast<const u32x4*>(q + h0 * D + 8 * c);
    const unsigned qw = reinterpret_cast<const unsigned*>(q + (h0 + 2 * w) * D)[lane];
    LAS f32x4* sc = (LAS f32x4*)(lds + SCB + w * SCW);
    {
      constexpr int NU = 32 - SEG;
      const int i = lane >> 2;
      const int sl = i < 4 * NU ? (i / NU) * 32 + SEG + i % NU : 128 + SOLO + (i - 4 * NU);
      sc[sl * 4 + (lane & 3)] = f32x4{-INFINITY, -INFINITY, -INFINITY, -INFINITY};
    }
    const LAS u32x4* ring = (const LAS u32x4*)lds + w * 256 + lane;
    u32x4 x[4];
    int j = 0;
#pragma unroll 1
    for (int seg = 0; seg < 5; ++seg) {
      const int n = seg < 4 ? SEG : SOLO;
      LAS f32x4* sp = sc + seg * 128 + r;
      float m = -INFINITY;
#pragma unroll 1
      for (int i0 = 0; i0 < n; ++i0, ++j) {
        lds_bar();
#pragma unroll
        for (int i = 0; i < 4; ++i) x[i] = ring[(j & 3) * 1024 + i * 64];
        f32x4 s;
#pragma unroll
        for (int i = 0; i < 4; ++i) {
          float d = dot2(x[i][0], qp[0], 0.f);
          d = dot2(x[i][1], qp[1], d); d = dot2(x[i][2], qp[2], d); d = dot2(x[i][3], qp[3], d);
          s[i] = row16_sum(d);
          m = fmaxf(m, s[i]);
        }
        if (c == 0) sp[i0 * 4] = s;
        if (j == 0) ((LAS unsigned*)(lds + QB))[w * 64 + lane] = qw;
      }
      ((LAS float*)(lds + MXB))[seg * 4 + w] = xrow_max(m);
      qp = ((const LAS u32x4*)(lds + QB))[(seg < 3 ? seg + 1 : mem) * 16 + c];
    }
#pragma unroll 1
    for (int seg = 0; seg < 5; ++seg) {
      const int n = seg < 4 ? SEG : SOLO;
      const LAS f32x4* sp = sc + seg * 128 + r;
      f32x2 acc[4];
#pragma unroll
      for (int i = 0; i < 4; ++i) acc[i] = f32x2{0.f, 0.f};
#pragma unroll 1
      for (int i0 = 0; i0 < n; ++i0, ++j) {
        lds_bar();
#pragma unroll
        for (int i = 0; i < 4; ++i) x[i] = ring[(j & 3) * 1024 + i * 64];
        if (i0 == 0) {
          const bool own = seg == mem || seg == 4;
          const f32x4 ma = ((const LAS f32x4*)(lds + MXB))[own ? mem : seg];
          const f32x4 mb = ((const LAS f32x4*)(lds + MXB))[own ? 4 : seg];
          const float mc = -(C2 * fmaxf(max4(ma), max4(mb)));
          LAS f32x4* e = sc + seg * 128 + lane;
          float l = 0.f;
#pragma unroll 1
          for (int y = 0; y < (seg < 4 ? 2 : 1); ++y) {
            f32x4 t = e[y * 64];
#pragma unroll
            for (int i = 0; i < 4; ++i) t[i] = __builtin_amdgcn_exp2f(fmaf(t[i], C2, mc));
            l += sum4(t);
            e[y * 64] = t;
          }
          l = row16_sum(l); l = swap32_add(l, l); l = swap16_add(l, l);
          ((LAS float*)(lds + LSB))[seg * 4 + w] = l;
        }
        const f32x4 p = sp[i0 * 4];
#pragma unroll
        for (int i = 0; i < 4; ++i)
#pragma unroll
          for (int e = 0; e < 4; ++e) {
            const f32x2 vv = {__builtin_bit_cast(float, x[i][e] << 16), __builtin_bit_cast(float, x[i][e] & 0xffff0000u)};
            acc[e] = __builtin_elementwise_fma(f32x2{p[i], p[i]}, vv, acc[e]);
          }
      }
      const float u0 = swap32_add(acc[0][0], acc[2][0]), u1 = swap32_add(acc[0][1], acc[2][1]);
      const float u2 = swap32_add(acc[1][0], acc[3][0]), u3 = swap32_add(acc[1][1], acc[3][1]);
      ((LAS f32x2*)(lds + REDB))[w * 64 + 4 * c + r] = f32x2{swap16_add(u0, u2), swap16_add(u1, u3)};
    }
    lds_bar();
  }
}
}

extern "C" void kernel_launch(void* const* d_in, const int* in_sizes, int n_in, void* d_out, int out_size, void* d_ws,
                              size_t ws_size, hipStream_t stream) {
  if (n_in != 3 || in_sizes[0] != NHEAD * D || out_size != NHEAD * D || ws_size < WS_NEED) return;
  attn_decode<<<NHEAD, 448, 0, stream>>>((const uint16_t*)d_in[0], (const uint16_t*)d_in[1], (const uint16_t*)d_in[2],
                                         (uint16_t*)d_out, (u64*)d_ws);
}
```

```cpp
#include <hip/hip_runtime.h>
#include <stdint.h>

namespace {
typedef unsigned long long u64;
using u32x4 = __attribute__((ext_vector_type(4))) unsigned;
using f32x4 = __attribute__((ext_vector_type(4))) float;
using f32x2 = __attribute__((ext_vector_type(2))) float;
using bf16x2 = __attribute__((ext_vector_type(2))) __bf16;
using bf16x4 = __attribute__((ext_vector_type(4))) __bf16;
#define LAS __attribute__((address_space(3)))
typedef __attribute__((address_space(1))) unsigned gu32;
typedef __attribute__((address_space(1))) unsigned long long gu64;
typedef __attribute__((address_space(1))) u32x4 gu128;

constexpr int SK = 8192, D = 128, NHEAD = 256;
constexpr unsigned HEADB = SK * D * 2;
constexpr int NG = 256, GB = 16384, RINGB = 4 * GB;
constexpr int SOLO = 8, SEG = (128 - SOLO) / 4;
constexpr int PRE = 6;
constexpr int GA = 2, GB2 = 1, GC = 2, GD = 1;
constexpr int NCP = 32, CPI = NG / NCP, NPUB = 8, OFS = 8, RDL = 4, WDL = 3, I0 = 5;
constexpr float I0F = 4.7f, KP = 25.f, STALL = 150.f, TAU0 = 53.2f;
constexpr int SCW = 144 * 64;
constexpr int SCB = RINGB, REDB = SCB + 4 * SCW, QB = REDB + 4 * 512, MXB = QB + 1024, LSB = MXB + 80, RCB = LSB + 80,
              LDSB = RCB + 16;
constexpr int CST = 512 + 32, TWO = NCP * CST, SLO = TWO + 32 * 32;
constexpr size_t WS_NEED = (size_t)SLO * 8 + (size_t)768 * 640;
constexpr float C2 = 0.088388347648318440f * 1.4426950408889634f;
constexpr int SPIN_CAP = 1 << 21;
static_assert(SEG % 2 == 0 && 4 * SEG + SOLO == 128 && 4 * (32 - SEG) + 16 - SOLO == 16 && GA + GB2 + GC + GD + 1 < SOLO &&
              RDL + WDL <= CPI && LDSB <= 160 * 1024, "layout");

__device__ __forceinline__ float dot2(unsigned a, unsigned b, float acc) {
  return __builtin_amdgcn_fdot2_f32_bf16(__builtin_bit_cast(bf16x2, a), __builtin_bit_cast(bf16x2, b), acc, false);
}
template <int CTRL> __device__ __forceinline__ float dpp(float x) {
  return __builtin_bit_cast(float, __builtin_amdgcn_mov_dpp(__builtin_bit_cast(int, x), CTRL, 0xf, 0xf, true));
}
__device__ __forceinline__ float row16_sum(float x) {
  x += dpp<0xB1>(x); x += dpp<0x4E>(x); x += dpp<0x141>(x); x += dpp<0x128>(x); return x;
}
__device__ __forceinline__ float swap32_add(float a, float b) {
  auto t = __builtin_amdgcn_permlane32_swap(__float_as_uint(a), __float_as_uint(b), false, false);
  return __uint_as_float(t[0]) + __uint_as_float(t[1]);
}
__device__ __forceinline__ float swap16_add(float a, float b) {
  auto t = __builtin_amdgcn_permlane16_swap(__float_as_uint(a), __float_as_uint(b), false, false);
  return __uint_as_float(t[0]) + __uint_as_float(t[1]);
}
__device__ __forceinline__ float xrow_max(float x) {
  auto s = __builtin_amdgcn_permlane16_swap(__float_as_uint(x), __float_as_uint(x), false, false);
  x = fmaxf(__uint_as_float(s[0]), __uint_as_float(s[1]));
  auto t = __builtin_amdgcn_permlane32_swap(__float_as_uint(x), __float_as_uint(x), false, false);
  return fmaxf(__uint_as_float(t[0]), __uint_as_float(t[1]));
}
__device__ __forceinline__ float max4(f32x4 a) { return fmaxf(fmaxf(a[0], a[1]), fmaxf(a[2], a[3])); }
__device__ __forceinline__ float sum4(f32x4 a) { return (a[0] + a[1]) + (a[2] + a[3]); }
template <int W> __device__ __forceinline__ void waitvm() { asm volatile("s_waitcnt vmcnt(%0)" ::"n"(W) : "memory"); }
__device__ __forceinline__ void bar() { asm volatile("s_barrier" ::: "memory"); }
__device__ __forceinline__ void lds_bar() { asm volatile("s_waitcnt lgkmcnt(0)\n\ts_barrier" ::: "memory"); }
__device__ __forceinline__ void bars(int n) {
#pragma unroll 1
  for (; n > 0; --n) bar();
}
__device__ __forceinline__ void st128(gu128* p, u32x4 x) {
  asm volatile("global_store_dwordx4 %0, %1, off sc1\n\ts_nop 1" ::"v"(p), "v"(x) : "memory");
}

#define TILE                                                              \
  "buffer_load_dwordx4 %[vo], s[60:63], s64 offen nt lds\n\t"             \
  "buffer_load_dwordx4 %[vo], s[60:63], s64 offen offset:1024 nt lds\n\t" \
  "buffer_load_dwordx4 %[vo], s[60:63], s64 offen offset:2048 nt lds\n\t" \
  "buffer_load_dwordx4 %[vo], s[60:63], s64 offen offset:3072 nt lds\n\t" \
  "s_add_u32 s64, s64, s72\n\ts_add_u32 m0, m0, s72\n\ts_cmp_lg_u32 s64, s65\n\t"
#define LEVEL(n) "2:\n\ts_waitcnt vmcnt(" #n ")\n\t" TILE "s_cbranch_scc1 2b\n\ts_branch 3f\n\t"
__device__ __forceinline__ void loader(int wave, u64 ka, u64 va, unsigned voff, unsigned ldsb, unsigned so0,
                                       unsigned sos, unsigned rc) {
  unsigned rn;
  asm volatile(
      "s_cmp_lg_u32 %[wv], 0\n\t"
      "s_cbranch_scc1 9f\n\t"
      "s_mov_b32 s60, %[kl]\n\ts_and_b32 s61, %[kh], 0xffff\n\ts_mov_b32 s62, %[nr]\n\ts_mov_b32 s63, 0x20000\n\t"
      "s_mov_b32 m0, %[lb]\n\ts_mov_b32 s64, %[so0]\n\ts_add_u32 s65, %[so0], %[pre]\n\ts_movk_i32 s72, 0x1000\n"
      "2:\n\t" TILE "s_cbranch_scc1 2b\n\t"
      "s_setprio 3\n\t"
      "s_add_u32 s65, %[so0], 0x8000\n\ts_movk_i32 s66, %[off0]\n\ts_mov_b32 s67, 0x18000\n\ts_mov_b32 s68, 0x18000\n\t"
      "s_movk_i32 s69, %[seg1]\n\ts_mov_b32 s70, 0\n\ts_mov_b32 s71, 1\n\ts_movk_i32 s73, 0x4000\n"
      "1:\n\t"
      "ds_read_b32 %[rn], %[rc]\n\t"
      "s_getpc_b64 vcc\n\ts_add_u32 vcc_lo, vcc_lo, s66\n\ts_addc_u32 vcc_hi, vcc_hi, 0\n\ts_setpc_b64 vcc\n\t"
      LEVEL(13) LEVEL(14) LEVEL(15) LEVEL(16) LEVEL(17) LEVEL(18) LEVEL(19) LEVEL(20) LEVEL(21) LEVEL(22) LEVEL(23)
      LEVEL(24) LEVEL(28)
      "3:\n\t"
      "s_add_u32 s64, s64, s67\n\ts_xor_b32 s67, s67, s68\n\t"
      "s_add_u32 s73, s73, 0x4000\n\ts_and_b32 s73, s73, 0xffff\n\ts_add_u32 m0, s73, %[lb]\n\t"
      "s_waitcnt lgkmcnt(0)\n\t"
      "v_med3_i32 %[rn], %[rn], 0, 11\n\tv_mad_u32_u24 %[rn], %[rn], 56, 12\n\t"
      "s_sub_u32 s69, s69, 1\n\ts_cmp_lg_u32 s69, 0\n\ts_cbranch_scc1 5f\n\t"
      "s_add_u32 s70, s70, 1\n\ts_movk_i32 s69, %[seg]\n\ts_add_u32 s64, s64, %[nxt]\n\t"
      "s_cmp_lt_u32 s70, 4\n\ts_cbranch_scc1 5f\n\t"
      "s_mov_b32 s64, %[sos]\n\ts_mov_b32 s68, 0\n\ts_movk_i32 s69, %[solo]\n\t"
      "s_cmp_eq_u32 s70, 4\n\ts_cbranch_scc1 5f\n\t"
      "s_mov_b32 s64, %[so0]\n\ts_mov_b32 s68, 0x18000\n\ts_movk_i32 s69, %[seg]\n\ts_mov_b32 s70, 0\n\t"
      "s_mov_b32 s60, %[vl]\n\ts_and_b32 s61, %[vh], 0xffff\n"
      "5:\n\t"
      "s_add_u32 s65, s64, 0x4000\n\t"
      "s_add_u32 s71, s71, 1\n\t"
      "s_cmp_lt_u32 s71, 3\n\ts_cbranch_scc1 1b\n\t"
      "s_barrier\n\t"
      "s_cmp_eq_u32 s71, 3\n\ts_cbranch_scc1 1b\n\t"
      "v_readfirstlane_b32 s66, %[rn]\n\t"
      "s_cmpk_lt_u32 s71, 255\n\ts_cbranch_scc1 1b\n\t"
      "s_movk_i32 s66, 684\n\t"
      "s_cmpk_eq_u32 s71, 255\n\ts_cbranch_scc1 1b\n\t"
      "s_waitcnt vmcnt(16)\n\ts_barrier\n\ts_waitcnt vmcnt(0)\n\ts_barrier\n\ts_barrier\n\ts_endpgm\n"
      "9:"
      : [rn] "=&v"(rn)
      : [wv] "s"(wave), [kl] "s"((unsigned)ka), [kh] "s"((unsigned)(ka >> 32)), [vl] "s"((unsigned)va),
        [vh] "s"((unsigned)(va >> 32)), [vo] "v"(voff), [lb] "s"(ldsb), [so0] "s"(so0), [sos] "s"(sos), [rc] "v"(rc),
        [nr] "n"(7 * HEADB), [pre] "n"(PRE * 4096), [off0] "n"(12 + 56 * I0), [seg1] "n"(SEG - 1), [seg] "n"(SEG),
        [solo] "n"(SOLO), [nxt] "n"(2 * HEADB - 4 * SEG * GB)
      : "vcc", "scc", "memory", "s60", "s61", "s62", "s63", "s64", "s65", "s66", "s67", "s68", "s69", "s70", "s71",
        "s72", "s73");
}

__global__ void __launch_bounds__(448, 2)
attn_decode(const uint16_t* __restrict__ q, const uint16_t* __restrict__ k, const uint16_t* __restrict__ v,
            uint16_t* __restrict__ out, u64* __restrict__ ws) {
  __shared__ __attribute__((aligned(16))) char lds_[LDSB];
  LAS char* lds = (LAS char*)lds_;
  volatile LAS int* rctl = (volatile LAS int*)(lds + RCB);
  const int bh = blockIdx.x;
  const int mem = (bh >> 1) & 3, h0 = bh & ~6;
  const int tid = threadIdx.x, lane = tid & 63, wave = __builtin_amdgcn_readfirstlane(tid >> 6);

  loader(wave, (u64)k + (size_t)h0 * HEADB, (u64)v + (size_t)h0 * HEADB, lane * 16, (unsigned)(size_t)lds,
         mem * 2 * GB, mem * 2 * HEADB + 4 * SEG * GB, (unsigned)(size_t)(lds + RCB));

  if (wave == 6) {
    gu32* tw = (gu32*)(ws + TWO + (bh >> 3) * 32);
    const int stg = (bh >> 3) & 15;
    bars(8 + stg);
    unsigned x0, wv;
    const unsigned one = 1, ack = 256;
    asm volatile("s_mov_b64 exec, 1\n\tglobal_atomic_add %0, %1, %2, off sc0\n\ts_mov_b64 exec, -1"
                 : "=&v"(x0) : "v"(tw), "v"(one) : "memory");
    bars(24);
    asm volatile("s_waitcnt vmcnt(0)" : "+v"(x0)::"memory");
    const unsigned xs = __builtin_amdgcn_readfirstlane(x0);
    unsigned tag = xs;
    bool acked = false, bad = false;
#pragma unroll 1
    for (int it = 0; tag - xs - 257u >= 8u; ++it) {
      asm volatile("global_load_dword %0, %1, off sc1" : "=v"(wv) : "v"(tw) : "memory");
      if (it < 2) bars(4);
      asm volatile("s_waitcnt vmcnt(0)" : "+v"(wv)::"memory");
      tag = __builtin_amdgcn_readfirstlane(wv);
      if (tag - xs == 8 && !acked) {
        acked = true;
        if (lane == 0) asm volatile("global_atomic_add %0, %1, off" ::"v"(tw), "v"(ack) : "memory");
      }
      if (it == 0) bars(tag - xs - 257u >= 8u ? 28 : 32);
      else if (it > 1) __builtin_amdgcn_s_sleep(8);
      if (it > SPIN_CAP) { bad = true; break; }
    }
    int nb = 68 + stg;
    const int L = lane & 31;
    const LAS f32x4* red = (const LAS f32x4*)(lds + REDB) + L;
    const LAS f32x4* mx = (const LAS f32x4*)(lds + MXB);
    const LAS f32x4* ls = (const LAS f32x4*)(lds + LSB);
    gu128* sl0 = (gu128*)(ws + SLO) + ((bh >> 3) * 2 + (bh & 1)) * 120;
    f32x4 oown = {0.f, 0.f, 0.f, 0.f};
    u32x4 a0 = {0, 0, 0, 0}, a1 = a0, a2 = a0, b0 = a0, b1 = a0, b2 = a0;
#define LOAD3(x0, x1, x2, p)                                                                               \
  asm volatile("global_load_dwordx4 %0, %3, off sc1\n\tglobal_load_dwordx4 %1, %3, off offset:640 sc1\n\t" \
               "global_load_dwordx4 %2, %3, off offset:1280 sc1"                                           \
               : "=&v"(x0), "=&v"(x1), "=&v"(x2) : "v"(p) : "memory")
#pragma unroll 1
    for (int u = 0; u < 4; ++u) {
      const int E = 128 + SEG * (u + 1) + 1;
      bars(E - nb);
      f32x4 o = (red[0] + red[32]) + (red[64] + red[96]);
      u32x4 fl = {__float_as_uint(C2 * max4(mx[u])), tag, __float_as_uint(sum4(ls[u])), tag};
      asm volatile("s_waitcnt lgkmcnt(0)" : "+v"(o), "+v"(fl)::"memory");
      gu128* sl = sl0 + u * (64 * 120);
      if (u != mem) {
        sl += ((mem - u - 1) & 3) * 40;
        if (lane < 32) st128(sl + lane, __builtin_bit_cast(u32x4, o));
        bars(GA);
        waitvm<0>();
        if (lane < 8 && !bad) st128(sl + 32 + lane, fl);
        nb = E + GA;
      } else {
        oown = o;
        bars(GA + GB2);
#pragma unroll 1
        for (int it = 0;; ++it) {
          LOAD3(b0, b1, b2, sl + 32);
          if (it == 0) bars(GC);
          asm volatile("s_waitcnt vmcnt(0)" : "+v"(b0), "+v"(b1), "+v"(b2)::"memory");
          const bool ok = b0[1] == tag && b0[3] == tag && b1[1] == tag && b1[3] == tag && b2[1] == tag && b2[3] == tag;
          if (__all(ok)) break;
          if (bad || it > SPIN_CAP) { bad = true; break; }
          __builtin_amdgcn_s_sleep(4);
        }
        LOAD3(a0, a1, a2, sl + L);
        bars(GD);
        asm volatile("s_waitcnt vmcnt(0)" : "+v"(a0), "+v"(a1), "+v"(a2)::"memory");
        nb = E + GA + GB2 + GC + GD;
      }
    }
#undef LOAD3
    float mo = C2 * fmaxf(max4(mx[mem]), max4(mx[4]));
    asm volatile("" : "+v"(mo));
    const float lo = sum4(ls[mem]) + sum4(ls[4]);
    const float m0 = __uint_as_float(b0[0]), m1 = __uint_as_float(b1[0]), m2 = __uint_as_float(b2[0]);
    const float M = fmaxf(fmaxf(mo, m0), fmaxf(m1, m2));
    const float wo = __builtin_amdgcn_exp2f(mo - M), w0 = __builtin_amdgcn_exp2f(m0 - M);
    const float w1 = __builtin_amdgcn_exp2f(m1 - M), w2 = __builtin_amdgcn_exp2f(m2 - M);
    float inv = 1.f / (fmaf(wo, lo, w0 * __uint_as_float(b0[2])) + fmaf(w1, __uint_as_float(b1[2]), w2 * __uint_as_float(b2[2])));
    if (bad) inv = __builtin_nanf("");
    f32x4 pre = (oown * wo + __builtin_bit_cast(f32x4, a0) * w0) +
                (__builtin_bit_cast(f32x4, a1) * w1 + __builtin_bit_cast(f32x4, a2) * w2);
    pre *= inv;
    float cs = wo * inv;
    gu64* op = (gu64*)(out + bh * D) + L;
    asm volatile("s_waitcnt lgkmcnt(0)" : "+v"(pre), "+v"(cs), "+v"(op)::"memory");
    bars(257 - nb);
    const f32x4 f = pre + ((red[0] + red[32]) + (red[64] + red[96])) * cs;
    if (lane < 32) *op = __builtin_bit_cast(u64, __builtin_convertvector(f, bf16x4));
  } else if (wave == 5) {
    rctl[0] = I0;
    gu64* cw = (gu64*)ws;
    u64 basev;
    asm volatile("global_load_dwordx2 %0, %1, off sc1" : "=v"(basev) : "v"(cw + (lane & (NCP - 1)) * CST) : "memory");
    unsigned tref = 0, tprev = 0, tnow = 0;
    float sch = 0.f, tau = 0.f, iv1 = 1e9f, fprev = 0.f, lagf = 0.f, lagp = 0.f, stl = 0.f;
    bool on = false;
    auto steps = [&](int n) {
#pragma unroll 1
      for (; n > 0; --n) {
        lds_bar();
        tnow = (unsigned)__builtin_amdgcn_s_memrealtime();
        sch += tau;
        if (on) {
          float lag = (float)(int)(tnow - tref) - sch;
          if (lag - lagp > STALL) { const float jump = lag - lagp; sch += jump; stl += jump; lag = lagp; }
          lagp = lag;
          lagf += (lag - lagf) * 0.25f;
          rctl[0] = (int)rintf(fminf(fmaxf(I0F + lagf * (1.f / KP), 0.f), 11.f));
        }
      }
    };
    steps(OFS - 2);
    asm volatile("s_waitcnt vmcnt(0)" : "+v"(basev)::"memory");
#pragma unroll 1
    for (int cp = 1; cp < NCP; ++cp) {
      const unsigned town = tnow;
      stl = 0.f;
      const u64 addv = (1ull << 40) + town;
      if (bh < NPUB && lane == 0)
        asm volatile("global_atomic_add_x2 %0, %1, off" ::"v"(cw + cp * CST), "v"(addv) : "memory");
      steps(RDL);
      const unsigned trd = tnow;
      u64 rdv;
      asm volatile("global_load_dwordx2 %0, %1, off sc1" : "=v"(rdv) : "v"(cw + cp * CST) : "memory");
      steps(WDL);
      asm volatile("s_waitcnt vmcnt(0)" : "+v"(rdv)::"memory");
      const unsigned blo = __builtin_amdgcn_readlane((unsigned)basev, cp);
      const unsigned bhi = __builtin_amdgcn_readlane((unsigned)(basev >> 32), cp);
      const u64 d = rdv - (((u64)bhi << 32) | blo);
      const unsigned cnt = min((unsigned)(d >> 40), (unsigned)NPUB);
      const int rel = (int)((unsigned)d - cnt * town) + (int)((NPUB - cnt) * (trd - town));
      const float frac = (float)rel * (1.f / NPUB);
      const float iv = (float)(int)(town - tprev) + (frac - fprev);
      const float cur = sch + (float)(int)(tref - town);
      tau = cp > 2 ? fminf(iv, iv1) * (1.f / CPI) : TAU0;
      float nw = frac + stl + tau * (RDL + WDL);
      if (on) nw = cur + fminf(fmaxf(nw - cur, -40.f), 40.f);
      else lagf = -frac;
      tref = town;
      sch = nw;
      lagp = (float)(int)(tnow - town) - nw;
      on = true;
      iv1 = iv; tprev = town; fprev = frac;
      steps(CPI - RDL - WDL);
    }
    steps(CPI - OFS);
    bars(3);
  } else {
    const int w = wave - 1, c = lane & 15, r = lane >> 4;
    u32x4 qp = *reinterpret_cast<const u32x4*>(q + h0 * D + 8 * c);
    const unsigned qw = reinterpret_cast<const unsigned*>(q + (h0 + 2 * w) * D)[lane];
    LAS f32x4* sc = (LAS f32x4*)(lds + SCB + w * SCW);
    {
      constexpr int NU = 32 - SEG;
      const int i = lane >> 2;
      const int sl = i < 4 * NU ? (i / NU) * 32 + SEG + i % NU : 128 + SOLO + (i - 4 * NU);
      sc[sl * 4 + (lane & 3)] = f32x4{-INFINITY, -INFINITY, -INFINITY, -INFINITY};
    }
    const LAS u32x4* ring = (const LAS u32x4*)lds + w * 256 + lane;
    u32x4 x[4];
    int j = 0;
#pragma unroll 1
    for (int seg = 0; seg < 5; ++seg) {
      const int n = seg < 4 ? SEG : SOLO;
      LAS f32x4* sp = sc + seg * 128 + r;
      float m = -INFINITY;
#pragma unroll 1
      for (int i0 = 0; i0 < n; ++i0, ++j) {
        lds_bar();
#pragma unroll
        for (int i = 0; i < 4; ++i) x[i] = ring[(j & 3) * 1024 + i * 64];
        f32x4 s;
#pragma unroll
        for (int i = 0; i < 4; ++i) {
          float d = dot2(x[i][0], qp[0], 0.f);
          d = dot2(x[i][1], qp[1], d); d = dot2(x[i][2], qp[2], d); d = dot2(x[i][3], qp[3], d);
          s[i] = row16_sum(d);
          m = fmaxf(m, s[i]);
        }
        if (c == 0) sp[i0 * 4] = s;
        if (j == 0) ((LAS unsigned*)(lds + QB))[w * 64 + lane] = qw;
      }
      ((LAS float*)(lds + MXB))[seg * 4 + w] = xrow_max(m);
      qp = ((const LAS u32x4*)(lds + QB))[(seg < 3 ? seg + 1 : mem) * 16 + c];
    }
#pragma unroll 1
    for (int seg = 0; seg < 5; ++seg) {
      const int n = seg < 4 ? SEG : SOLO;
      const LAS f32x4* sp = sc + seg * 128 + r;
      f32x2 acc[4];
#pragma unroll
      for (int i = 0; i < 4; ++i) acc[i] = f32x2{0.f, 0.f};
#pragma unroll 1
      for (int i0 = 0; i0 < n; ++i0, ++j) {
        lds_bar();
#pragma unroll
        for (int i = 0; i < 4; ++i) x[i] = ring[(j & 3) * 1024 + i * 64];
        if (i0 == 0) {
          const bool own = seg == mem || seg == 4;
          const f32x4 ma = ((const LAS f32x4*)(lds + MXB))[own ? mem : seg];
          const f32x4 mb = ((const LAS f32x4*)(lds + MXB))[own ? 4 : seg];
          const float mc = -(C2 * fmaxf(max4(ma), max4(mb)));
          LAS f32x4* e = sc + seg * 128 + lane;
          float l = 0.f;
#pragma unroll 1
          for (int y = 0; y < (seg < 4 ? 2 : 1); ++y) {
            f32x4 t = e[y * 64];
#pragma unroll
            for (int i = 0; i < 4; ++i) t[i] = __builtin_amdgcn_exp2f(fmaf(t[i], C2, mc));
            l += sum4(t);
            e[y * 64] = t;
          }
          l = row16_sum(l); l = swap32_add(l, l); l = swap16_add(l, l);
          ((LAS float*)(lds + LSB))[seg * 4 + w] = l;
          asm volatile("" ::: "memory");
        }
        const f32x4 p = sp[i0 * 4];
#pragma unroll
        for (int i = 0; i < 4; ++i)
#pragma unroll
          for (int e = 0; e < 4; ++e) {
            const f32x2 vv = {__builtin_bit_cast(float, x[i][e] << 16), __builtin_bit_cast(float, x[i][e] & 0xffff0000u)};
            acc[e] = __builtin_elementwise_fma(f32x2{p[i], p[i]}, vv, acc[e]);
          }
      }
      const float u0 = swap32_add(acc[0][0], acc[2][0]), u1 = swap32_add(acc[0][1], acc[2][1]);
      const float u2 = swap32_add(acc[1][0], acc[3][0]), u3 = swap32_add(acc[1][1], acc[3][1]);
      ((LAS f32x2*)(lds + REDB))[w * 64 + 4 * c + r] = f32x2{swap16_add(u0, u2), swap16_add(u1, u3)};
    }
    lds_bar();
  }
}
}

extern "C" void kernel_launch(void* const* d_in, const int* in_sizes, int n_in, void* d_out, int out_size, void* d_ws,
                              size_t ws_size, hipStream_t stream) {
  if (n_in != 3 || in_sizes[0] != NHEAD * D || out_size != NHEAD * D || ws_size < WS_NEED) return;
  attn_decode<<<NHEAD, 448, 0, stream>>>((const uint16_t*)d_in[0], (const uint16_t*)d_in[1], (const uint16_t*)d_in[2],
                                         (uint16_t*)d_out, (u64*)d_ws);
}
```

```cpp
#include <hip/hip_runtime.h>
#include <stdint.h>

namespace {
typedef unsigned long long u64;
using u32x4 = __attribute__((ext_vector_type(4))) unsigned;
using f32x4 = __attribute__((ext_vector_type(4))) float;
using f32x2 = __attribute__((ext_vector_type(2))) float;
using bf16x2 = __attribute__((ext_vector_type(2))) __bf16;
using bf16x4 = __attribute__((ext_vector_type(4))) __bf16;
#define LAS __attribute__((address_space(3)))
typedef __attribute__((address_space(1))) unsigned gu32;
typedef __attribute__((address_space(1))) unsigned long long gu64;
typedef __attribute__((address_space(1))) u32x4 gu128;

constexpr int SK = 8192, D = 128, NHEAD = 256;
constexpr unsigned HEADB = SK * D * 2;
constexpr int NG = 256, GB = 16384, RINGB = 4 * GB;
constexpr int SOLO = 8, SEG = (128 - SOLO) / 4;
constexpr int PRE = 6;
constexpr int GA = 2, GB2 = 1, GC = 2, GD = 1;
constexpr int NCP = 32, CPI = NG / NCP, NPUB = 8, OFS = 8, RDL = 4, WDL = 3, I0 = 5;
constexpr float I0F = 4.5f, KP = 30.f, STALL = 150.f, TAU0 = 53.2f;
constexpr int SCW = 144 * 64;
constexpr int SCB = RINGB, REDB = SCB + 4 * SCW, QB = REDB + 4 * 512, MXB = QB + 1024, LSB = MXB + 80, RCB = LSB + 80,
              LDSB = RCB + 16;
constexpr int CST = 512 + 32, TWO = NCP * CST, SLO = TWO + 32 * 32;
constexpr size_t WS_NEED = (size_t)SLO * 8 + (size_t)768 * 640;
constexpr float C2 = 0.088388347648318440f * 1.4426950408889634f;
constexpr int SPIN_CAP = 1 << 21;
static_assert(SEG % 2 == 0 && 4 * SEG + SOLO == 128 && 4 * (32 - SEG) + 16 - SOLO == 16 && GA + GB2 + GC + GD + 1 < SOLO &&
              RDL + WDL <= CPI && LDSB <= 160 * 1024, "layout");

__device__ __forceinline__ float dot2(unsigned a, unsigned b, float acc) {
  return __builtin_amdgcn_fdot2_f32_bf16(__builtin_bit_cast(bf16x2, a), __builtin_bit_cast(bf16x2, b), acc, false);
}
template <int CTRL> __device__ __forceinline__ float dpp(float x) {
  return __builtin_bit_cast(float, __builtin_amdgcn_mov_dpp(__builtin_bit_cast(int, x), CTRL, 0xf, 0xf, true));
}
__device__ __forceinline__ float row16_sum(float x) {
  x += dpp<0xB1>(x); x += dpp<0x4E>(x); x += dpp<0x141>(x); x += dpp<0x128>(x); return x;
}
__device__ __forceinline__ float swap32_add(float a, float b) {
  auto t = __builtin_amdgcn_permlane32_swap(__float_as_uint(a), __float_as_uint(b), false, false);
  return __uint_as_float(t[0]) + __uint_as_float(t[1]);
}
__device__ __forceinline__ float swap16_add(float a, float b) {
  auto t = __builtin_amdgcn_permlane16_swap(__float_as_uint(a), __float_as_uint(b), false, false);
  return __uint_as_float(t[0]) + __uint_as_float(t[1]);
}
__device__ __forceinline__ float xrow_max(float x) {
  auto s = __builtin_amdgcn_permlane16_swap(__float_as_uint(x), __float_as_uint(x), false, false);
  x = fmaxf(__uint_as_float(s[0]), __uint_as_float(s[1]));
  auto t = __builtin_amdgcn_permlane32_swap(__float_as_uint(x), __float_as_uint(x), false, false);
  return fmaxf(__uint_as_float(t[0]), __uint_as_float(t[1]));
}
__device__ __forceinline__ float max4(f32x4 a) { return fmaxf(fmaxf(a[0], a[1]), fmaxf(a[2], a[3])); }
__device__ __forceinline__ float sum4(f32x4 a) { return (a[0] + a[1]) + (a[2] + a[3]); }
template <int W> __device__ __forceinline__ void waitvm() { asm volatile("s_waitcnt vmcnt(%0)" ::"n"(W) : "memory"); }
__device__ __forceinline__ void bar() { asm volatile("s_barrier" ::: "memory"); }
__device__ __forceinline__ void lds_bar() { asm volatile("s_waitcnt lgkmcnt(0)\n\ts_barrier" ::: "memory"); }
__device__ __forceinline__ void bars(int n) {
#pragma unroll 1
  for (; n > 0; --n) bar();
}
__device__ __forceinline__ void st128(gu128* p, u32x4 x) {
  asm volatile("global_store_dwordx4 %0, %1, off sc1\n\ts_nop 1" ::"v"(p), "v"(x) : "memory");
}

#define TILE                                                              \
  "buffer_load_dwordx4 %[vo], s[60:63], s64 offen nt lds\n\t"             \
  "buffer_load_dwordx4 %[vo], s[60:63], s64 offen offset:1024 nt lds\n\t" \
  "buffer_load_dwordx4 %[vo], s[60:63], s64 offen offset:2048 nt lds\n\t" \
  "buffer_load_dwordx4 %[vo], s[60:63], s64 offen offset:3072 nt lds\n\t" \
  "s_add_u32 s64, s64, s72\n\ts_add_u32 m0, m0, s72\n\ts_cmp_lg_u32 s64, s65\n\t"
#define LEVEL(n) "2:\n\ts_waitcnt vmcnt(" #n ")\n\t" TILE "s_cbranch_scc1 2b\n\ts_branch 3f\n\t"
__device__ __forceinline__ void loader(int wave, u64 ka, u64 va, unsigned voff, unsigned ldsb, unsigned so0,
                                       unsigned sos, unsigned rc) {
  unsigned rn;
  asm volatile(
      "s_cmp_lg_u32 %[wv], 0\n\t"
      "s_cbranch_scc1 9f\n\t"
      "s_mov_b32 s60, %[kl]\n\ts_and_b32 s61, %[kh], 0xffff\n\ts_mov_b32 s62, %[nr]\n\ts_mov_b32 s63, 0x20000\n\t"
      "s_mov_b32 m0, %[lb]\n\ts_mov_b32 s64, %[so0]\n\ts_add_u32 s65, %[so0], %[pre]\n\ts_movk_i32 s72, 0x1000\n"
      "2:\n\t" TILE "s_cbranch_scc1 2b\n\t"
      "s_setprio 3\n\t"
      "s_add_u32 s65, %[so0], 0x8000\n\ts_movk_i32 s66, %[off0]\n\ts_mov_b32 s67, 0x18000\n\ts_mov_b32 s68, 0x18000\n\t"
      "s_movk_i32 s69, %[seg1]\n\ts_mov_b32 s70, 0\n\ts_mov_b32 s71, 1\n\ts_movk_i32 s73, 0x4000\n"
      "1:\n\t"
      "ds_read_b32 %[rn], %[rc]\n\t"
      "s_getpc_b64 vcc\n\ts_add_u32 vcc_lo, vcc_lo, s66\n\ts_addc_u32 vcc_hi, vcc_hi, 0\n\ts_setpc_b64 vcc\n\t"
      LEVEL(13) LEVEL(14) LEVEL(15) LEVEL(16) LEVEL(17) LEVEL(18) LEVEL(19) LEVEL(20) LEVEL(21) LEVEL(22) LEVEL(23)
      LEVEL(24) LEVEL(28)
      "3:\n\t"
      "s_add_u32 s64, s64, s67\n\ts_xor_b32 s67, s67, s68\n\t"
      "s_add_u32 s73, s73, 0x4000\n\ts_and_b32 s73, s73, 0xffff\n\ts_add_u32 m0, s73, %[lb]\n\t"
      "s_waitcnt lgkmcnt(0)\n\t"
      "v_med3_i32 %[rn], %[rn], 0, 11\n\tv_mad_u32_u24 %[rn], %[rn], 56, 12\n\t"
      "s_sub_u32 s69, s69, 1\n\ts_cmp_lg_u32 s69, 0\n\ts_cbranch_scc1 5f\n\t"
      "s_add_u32 s70, s70, 1\n\ts_movk_i32 s69, %[seg]\n\ts_add_u32 s64, s64, %[nxt]\n\t"
      "s_cmp_lt_u32 s70, 4\n\ts_cbranch_scc1 5f\n\t"
      "s_mov_b32 s64, %[sos]\n\ts_mov_b32 s68, 0\n\ts_movk_i32 s69, %[solo]\n\t"
      "s_cmp_eq_u32 s70, 4\n\ts_cbranch_scc1 5f\n\t"
      "s_mov_b32 s64, %[so0]\n\ts_mov_b32 s68, 0x18000\n\ts_movk_i32 s69, %[seg]\n\ts_mov_b32 s70, 0\n\t"
      "s_mov_b32 s60, %[vl]\n\ts_and_b32 s61, %[vh], 0xffff\n"
      "5:\n\t"
      "s_add_u32 s65, s64, 0x4000\n\t"
      "s_add_u32 s71, s71, 1\n\t"
      "s_cmp_lt_u32 s71, 3\n\ts_cbranch_scc1 1b\n\t"
      "s_barrier\n\t"
      "s_cmp_eq_u32 s71, 3\n\ts_cbranch_scc1 1b\n\t"
      "v_readfirstlane_b32 s66, %[rn]\n\t"
      "s_cmpk_lt_u32 s71, 255\n\ts_cbranch_scc1 1b\n\t"
      "s_movk_i32 s66, 684\n\t"
      "s_cmpk_eq_u32 s71, 255\n\ts_cbranch_scc1 1b\n\t"
      "s_waitcnt vmcnt(16)\n\ts_barrier\n\ts_waitcnt vmcnt(0)\n\ts_barrier\n\ts_barrier\n\ts_endpgm\n"
      "9:"
      : [rn] "=&v"(rn)
      : [wv] "s"(wave), [kl] "s"((unsigned)ka), [kh] "s"((unsigned)(ka >> 32)), [vl] "s"((unsigned)va),
        [vh] "s"((unsigned)(va >> 32)), [vo] "v"(voff), [lb] "s"(ldsb), [so0] "s"(so0), [sos] "s"(sos), [rc] "v"(rc),
        [nr] "n"(7 * HEADB), [pre] "n"(PRE * 4096), [off0] "n"(12 + 56 * I0), [seg1] "n"(SEG - 1), [seg] "n"(SEG),
        [solo] "n"(SOLO), [nxt] "n"(2 * HEADB - 4 * SEG * GB)
      : "vcc", "scc", "memory", "s60", "s61", "s62", "s63", "s64", "s65", "s66", "s67", "s68", "s69", "s70", "s71",
        "s72", "s73");
}

__global__ void __launch_bounds__(448, 2)
attn_decode(const uint16_t* __restrict__ q, const uint16_t* __restrict__ k, const uint16_t* __restrict__ v,
            uint16_t* __restrict__ out, u64* __restrict__ ws) {
  __shared__ __attribute__((aligned(16))) char lds_[LDSB];
  LAS char* lds = (LAS char*)lds_;
  volatile LAS int* rctl = (volatile LAS int*)(lds + RCB);
  const int bid = blockIdx.x;
  const int mem = ((bid >> 1) + (bid >> 4)) & 3;
  const int h0 = (bid & ~7) + ((bid ^ (bid >> 3)) & 1);
  const int bh = h0 + 2 * mem;
  const int tid = threadIdx.x, lane = tid & 63, wave = __builtin_amdgcn_readfirstlane(tid >> 6);

  loader(wave, (u64)k + (size_t)h0 * HEADB, (u64)v + (size_t)h0 * HEADB, lane * 16, (unsigned)(size_t)lds,
         mem * 2 * GB, mem * 2 * HEADB + 4 * SEG * GB, (unsigned)(size_t)(lds + RCB));

  if (wave == 6) {
    gu32* tw = (gu32*)(ws + TWO + (bid >> 3) * 32);
    const int stg = (bid >> 3) & 15;
    bars(8 + stg);
    unsigned x0, wv;
    const unsigned one = 1, ack = 256;
    asm volatile("s_mov_b64 exec, 1\n\tglobal_atomic_add %0, %1, %2, off sc0\n\ts_mov_b64 exec, -1"
                 : "=&v"(x0) : "v"(tw), "v"(one) : "memory");
    bars(24);
    asm volatile("s_waitcnt vmcnt(0)" : "+v"(x0)::"memory");
    const unsigned xs = __builtin_amdgcn_readfirstlane(x0);
    unsigned tag = xs;
    bool acked = false, bad = false;
#pragma unroll 1
    for (int it = 0; tag - xs - 257u >= 8u; ++it) {
      asm volatile("global_load_dword %0, %1, off sc1" : "=v"(wv) : "v"(tw) : "memory");
      if (it < 2) bars(4);
      asm volatile("s_waitcnt vmcnt(0)" : "+v"(wv)::"memory");
      tag = __builtin_amdgcn_readfirstlane(wv);
      if (tag - xs == 8 && !acked) {
        acked = true;
        if (lane == 0) asm volatile("global_atomic_add %0, %1, off" ::"v"(tw), "v"(ack) : "memory");
      }
      if (it == 0) bars(tag - xs - 257u >= 8u ? 28 : 32);
      else if (it > 1) __builtin_amdgcn_s_sleep(8);
      if (it > SPIN_CAP) { bad = true; break; }
    }
    int nb = 68 + stg;
    const int L = lane & 31;
    const LAS f32x4* red = (const LAS f32x4*)(lds + REDB) + L;
    const LAS f32x4* mx = (const LAS f32x4*)(lds + MXB);
    const LAS f32x4* ls = (const LAS f32x4*)(lds + LSB);
    gu128* sl0 = (gu128*)(ws + SLO) + ((bid >> 3) * 2 + (bid & 1)) * 120;
    f32x4 oown = {0.f, 0.f, 0.f, 0.f};
    u32x4 a0 = {0, 0, 0, 0}, a1 = a0, a2 = a0, b0 = a0, b1 = a0, b2 = a0;
#define LOAD3(x0, x1, x2, p)                                                                               \
  asm volatile("global_load_dwordx4 %0, %3, off sc1\n\tglobal_load_dwordx4 %1, %3, off offset:640 sc1\n\t" \
               "global_load_dwordx4 %2, %3, off offset:1280 sc1"                                           \
               : "=&v"(x0), "=&v"(x1), "=&v"(x2) : "v"(p) : "memory")
#pragma unroll 1
    for (int u = 0; u < 4; ++u) {
      const int E = 128 + SEG * (u + 1) + 1;
      bars(E - nb);
      f32x4 o = (red[0] + red[32]) + (red[64] + red[96]);
      u32x4 fl = {__float_as_uint(C2 * max4(mx[u])), tag, __float_as_uint(sum4(ls[u])), tag};
      asm volatile("s_waitcnt lgkmcnt(0)" : "+v"(o), "+v"(fl)::"memory");
      gu128* sl = sl0 + u * (64 * 120);
      if (u != mem) {
        sl += ((mem - u - 1) & 3) * 40;
        if (lane < 32) st128(sl + lane, __builtin_bit_cast(u32x4, o));
        bars(GA);
        waitvm<0>();
        if (lane < 8 && !bad) st128(sl + 32 + lane, fl);
        nb = E + GA;
      } else {
        oown = o;
        bars(GA + GB2);
#pragma unroll 1
        for (int it = 0;; ++it) {
          LOAD3(b0, b1, b2, sl + 32);
          if (it == 0) bars(GC);
          asm volatile("s_waitcnt vmcnt(0)" : "+v"(b0), "+v"(b1), "+v"(b2)::"memory");
          const bool ok = b0[1] == tag && b0[3] == tag && b1[1] == tag && b1[3] == tag && b2[1] == tag && b2[3] == tag;
          if (__all(ok)) break;
          if (bad || it > SPIN_CAP) { bad = true; break; }
          __builtin_amdgcn_s_sleep(4);
        }
        LOAD3(a0, a1, a2, sl + L);
        bars(GD);
        asm volatile("s_waitcnt vmcnt(0)" : "+v"(a0), "+v"(a1), "+v"(a2)::"memory");
        nb = E + GA + GB2 + GC + GD;
      }
    }
#undef LOAD3
    float mo = C2 * fmaxf(max4(mx[mem]), max4(mx[4]));
    asm volatile("" : "+v"(mo));
    const float lo = sum4(ls[mem]) + sum4(ls[4]);
    const float m0 = __uint_as_float(b0[0]), m1 = __uint_as_float(b1[0]), m2 = __uint_as_float(b2[0]);
    const float M = fmaxf(fmaxf(mo, m0), fmaxf(m1, m2));
    const float wo = __builtin_amdgcn_exp2f(mo - M), w0 = __builtin_amdgcn_exp2f(m0 - M);
    const float w1 = __builtin_amdgcn_exp2f(m1 - M), w2 = __builtin_amdgcn_exp2f(m2 - M);
    float inv = 1.f / (fmaf(wo, lo, w0 * __uint_as_float(b0[2])) + fmaf(w1, __uint_as_float(b1[2]), w2 * __uint_as_float(b2[2])));
    if (bad) inv = __builtin_nanf("");
    f32x4 pre = (oown * wo + __builtin_bit_cast(f32x4, a0) * w0) +
                (__builtin_bit_cast(f32x4, a1) * w1 + __builtin_bit_cast(f32x4, a2) * w2);
    pre *= inv;
    float cs = wo * inv;
    gu64* op = (gu64*)(out + bh * D) + L;
    asm volatile("s_waitcnt lgkmcnt(0)" : "+v"(pre), "+v"(cs), "+v"(op)::"memory");
    bars(257 - nb);
    const f32x4 f = pre + ((red[0] + red[32]) + (red[64] + red[96])) * cs;
    if (lane < 32) *op = __builtin_bit_cast(u64, __builtin_convertvector(f, bf16x4));
  } else if (wave == 5) {
    rctl[0] = I0;
    gu64* cw = (gu64*)ws;
    u64 basev;
    asm volatile("global_load_dwordx2 %0, %1, off sc1" : "=v"(basev) : "v"(cw + (lane & (NCP - 1)) * CST) : "memory");
    unsigned tref = 0, tprev = 0, tnow = 0;
    float sch = 0.f, tau = 0.f, fprev = 0.f, lagf = 0.f, lagp = 0.f, stl = 0.f;
    bool on = false;
    auto steps = [&](int n) {
#pragma unroll 1
      for (; n > 0; --n) {
        lds_bar();
        tnow = (unsigned)__builtin_amdgcn_s_memrealtime();
        sch += tau;
        if (on) {
          float lag = (float)(int)(tnow - tref) - sch;
          if (lag - lagp > STALL) { const float jump = lag - lagp; sch += jump; stl += jump; lag = lagp; }
          lagp = lag;
          lagf += (lag - lagf) * 0.25f;
          rctl[0] = (int)rintf(fminf(fmaxf(I0F + lagf * (1.f / KP), 0.f), 11.f));
        }
      }
    };
    steps(OFS - 2);
    asm volatile("s_waitcnt vmcnt(0)" : "+v"(basev)::"memory");
#pragma unroll 1
    for (int cp = 1; cp < NCP; ++cp) {
      const unsigned town = tnow;
      stl = 0.f;
      const u64 addv = (1ull << 40) + town;
      if ((((bid >> 3) - (bid & 7) * 4 - cp * 5) & 31) == 0 && lane == 0)
        asm volatile("global_atomic_add_x2 %0, %1, off" ::"v"(cw + cp * CST), "v"(addv) : "memory");
      steps(RDL);
      const unsigned trd = tnow;
      u64 rdv;
      asm volatile("global_load_dwordx2 %0, %1, off sc1" : "=v"(rdv) : "v"(cw + cp * CST) : "memory");
      steps(WDL);
      asm volatile("s_waitcnt vmcnt(0)" : "+v"(rdv)::"memory");
      const unsigned blo = __builtin_amdgcn_readlane((unsigned)basev, cp);
      const unsigned bhi = __builtin_amdgcn_readlane((unsigned)(basev >> 32), cp);
      const u64 d = rdv - (((u64)bhi << 32) | blo);
      const unsigned cnt = min((unsigned)(d >> 40), (unsigned)NPUB);
      const int rel = (int)((unsigned)d - cnt * town) + (int)((NPUB - cnt) * (trd - town));
      const float frac = (float)rel * (1.f / NPUB);
      const float iv = (float)(int)(town - tprev) + (frac - fprev);
      const float cur = sch + (float)(int)(tref - town);
      tau = cp > 2 ? tau + 0.2f * fminf(fmaxf(iv * (1.f / CPI) - tau, -0.6f), 0.6f) : TAU0;
      float nw = frac + stl + tau * (RDL + WDL);
      if (on) nw = cur + fminf(fmaxf(nw - cur, -40.f), 40.f);
      else lagf = -frac;
      tref = town;
      sch = nw;
      lagp = (float)(int)(tnow - town) - nw;
      on = true;
      tprev = town; fprev = frac;
      steps(CPI - RDL - WDL);
    }
    steps(CPI - OFS);
    bars(3);
  } else {
    const int w = wave - 1, c = lane & 15, r = lane >> 4;
    u32x4 qp = *reinterpret_cast<const u32x4*>(q + h0 * D + 8 * c);
    const unsigned qw = reinterpret_cast<const unsigned*>(q + (h0 + 2 * w) * D)[lane];
    LAS f32x4* sc = (LAS f32x4*)(lds + SCB + w * SCW);
    {
      constexpr int NU = 32 - SEG;
      const int i = lane >> 2;
      const int sl = i < 4 * NU ? (i / NU) * 32 + SEG + i % NU : 128 + SOLO + (i - 4 * NU);
      sc[sl * 4 + (lane & 3)] = f32x4{-INFINITY, -INFINITY, -INFINITY, -INFINITY};
    }
    const LAS u32x4* ring = (const LAS u32x4*)lds + w * 256 + lane;
    u32x4 x[4];
    int j = 0;
#pragma unroll 1
    for (int seg = 0; seg < 5; ++seg) {
      const int n = seg < 4 ? SEG : SOLO;
      LAS f32x4* sp = sc + seg * 128 + r;
      float m = -INFINITY;
#pragma unroll 1
      for (int i0 = 0; i0 < n; ++i0, ++j) {
        lds_bar();
#pragma unroll
        for (int i = 0; i < 4; ++i) x[i] = ring[(j & 3) * 1024 + i * 64];
        f32x4 s;
#pragma unroll
        for (int i = 0; i < 4; ++i) {
          float d = dot2(x[i][0], qp[0], 0.f);
          d = dot2(x[i][1], qp[1], d); d = dot2(x[i][2], qp[2], d); d = dot2(x[i][3], qp[3], d);
          s[i] = row16_sum(d);
          m = fmaxf(m, s[i]);
        }
        if (c == 0) sp[i0 * 4] = s;
        if (j == 0) ((LAS unsigned*)(lds + QB))[w * 64 + lane] = qw;
      }
      ((LAS float*)(lds + MXB))[seg * 4 + w] = xrow_max(m);
      qp = ((const LAS u32x4*)(lds + QB))[(seg < 3 ? seg + 1 : mem) * 16 + c];
    }
#pragma unroll 1
    for (int seg = 0; seg < 5; ++seg) {
      const int n = seg < 4 ? SEG : SOLO;
      const LAS f32x4* sp = sc + seg * 128 + r;
      f32x2 acc[4];
#pragma unroll
      for (int i = 0; i < 4; ++i) acc[i] = f32x2{0.f, 0.f};
#pragma unroll 1
      for (int i0 = 0; i0 < n; ++i0, ++j) {
        lds_bar();
#pragma unroll
        for (int i = 0; i < 4; ++i) x[i] = ring[(j & 3) * 1024 + i * 64];
        if (i0 == 0) {
          const bool own = seg == mem || seg == 4;
          const f32x4 ma = ((const LAS f32x4*)(lds + MXB))[own ? mem : seg];
          const f32x4 mb = ((const LAS f32x4*)(lds + MXB))[own ? 4 : seg];
          const float mc = -(C2 * fmaxf(max4(ma), max4(mb)));
          LAS f32x4* e = sc + seg * 128 + lane;
          float l = 0.f;
#pragma unroll 1
          for (int y = 0; y < (seg < 4 ? 2 : 1); ++y) {
            f32x4 t = e[y * 64];
#pragma unroll
            for (int i = 0; i < 4; ++i) t[i] = __builtin_amdgcn_exp2f(fmaf(t[i], C2, mc));
            l += sum4(t);
            e[y * 64] = t;
          }
          l = row16_sum(l); l = swap32_add(l, l); l = swap16_add(l, l);
          ((LAS float*)(lds + LSB))[seg * 4 + w] = l;
          asm volatile("" ::: "memory");
        }
        const f32x4 p = sp[i0 * 4];
#pragma unroll
        for (int i = 0; i < 4; ++i)
#pragma unroll
          for (int e = 0; e < 4; ++e) {
            const f32x2 vv = {__builtin_bit_cast(float, x[i][e] << 16), __builtin_bit_cast(float, x[i][e] & 0xffff0000u)};
            acc[e] = __builtin_elementwise_fma(f32x2{p[i], p[i]}, vv, acc[e]);
          }
      }
      const float u0 = swap32_add(acc[0][0], acc[2][0]), u1 = swap32_add(acc[0][1], acc[2][1]);
      const float u2 = swap32_add(acc[1][0], acc[3][0]), u3 = swap32_add(acc[1][1], acc[3][1]);
      ((LAS f32x2*)(lds + REDB))[w * 64 + 4 * c + r] = f32x2{swap16_add(u0, u2), swap16_add(u1, u3)};
    }
    lds_bar();
  }
}
}

extern "C" void kernel_launch(void* const* d_in, const int* in_sizes, int n_in, void* d_out, int out_size, void* d_ws,
                              size_t ws_size, hipStream_t stream) {
  if (n_in != 3 || in_sizes[0] != NHEAD * D || out_size != NHEAD * D || ws_size < WS_NEED) return;
  attn_decode<<<NHEAD, 448, 0, stream>>>((const uint16_t*)d_in[0], (const uint16_t*)d_in[1], (const uint16_t*)d_in[2],
                                         (uint16_t*)d_out, (u64*)d_ws);
}
```

```cpp
#include <hip/hip_runtime.h>
#include <stdint.h>

namespace {
typedef unsigned long long u64;
using u32x4 = __attribute__((ext_vector_type(4))) unsigned;
using f32x4 = __attribute__((ext_vector_type(4))) float;
using f32x2 = __attribute__((ext_vector_type(2))) float;
using bf16x2 = __attribute__((ext_vector_type(2))) __bf16;
using bf16x4 = __attribute__((ext_vector_type(4))) __bf16;
#define LAS __attribute__((address_space(3)))
typedef __attribute__((address_space(1))) unsigned gu32;
typedef __attribute__((address_space(1))) unsigned long long gu64;
typedef __attribute__((address_space(1))) u32x4 gu128;

constexpr int SK = 8192, D = 128, NHEAD = 256;
constexpr unsigned HEADB = SK * D * 2;
constexpr int NG = 256, GB = 16384, RINGB = 4 * GB;
constexpr int SOLO = 8, SEG = (128 - SOLO) / 4;
constexpr int PRE = 6;
constexpr int GA = 2, GB2 = 1, GC = 2, GD = 1;
constexpr int NCP = 32, CPI = NG / NCP, NPUB = 8, OFS = 8, RDL = 4, WDL = 3, I0 = 5;
constexpr float I0F = 4.5f, KP = 30.f, STALL = 150.f, TAU0 = 53.2f;
constexpr int SCW = 144 * 64;
constexpr int SCB = RINGB, REDB = SCB + 4 * SCW, QB = REDB + 4 * 512, MXB = QB + 1024, LSB = MXB + 80, RCB = LSB + 80,
              LDSB = RCB + 16;
constexpr int CST = 512 + 32, TWO = NCP * CST, SLO = TWO + 32 * 32;
constexpr size_t WS_NEED = (size_t)SLO * 8 + (size_t)768 * 640;
constexpr float C2 = 0.088388347648318440f * 1.4426950408889634f;
constexpr int SPIN_CAP = 1 << 21;
static_assert(SEG % 2 == 0 && 4 * SEG + SOLO == 128 && 4 * (32 - SEG) + 16 - SOLO == 16 && GA + GB2 + GC + GD + 1 < SOLO &&
              RDL + WDL <= CPI && LDSB <= 160 * 1024, "layout");

__device__ __forceinline__ float dot2(unsigned a, unsigned b, float acc) {
  return __builtin_amdgcn_fdot2_f32_bf16(__builtin_bit_cast(bf16x2, a), __builtin_bit_cast(bf16x2, b), acc, false);
}
template <int CTRL> __device__ __forceinline__ float dpp(float x) {
  return __builtin_bit_cast(float, __builtin_amdgcn_mov_dpp(__builtin_bit_cast(int, x), CTRL, 0xf, 0xf, true));
}
__device__ __forceinline__ float row16_sum(float x) {
  x += dpp<0xB1>(x); x += dpp<0x4E>(x); x += dpp<0x141>(x); x += dpp<0x128>(x); return x;
}
__device__ __forceinline__ float swap32_add(float a, float b) {
  auto t = __builtin_amdgcn_permlane32_swap(__float_as_uint(a), __float_as_uint(b), false, false);
  return __uint_as_float(t[0]) + __uint_as_float(t[1]);
}
__device__ __forceinline__ float swap16_add(float a, float b) {
  auto t = __builtin_amdgcn_permlane16_swap(__float_as_uint(a), __float_as_uint(b), false, false);
  return __uint_as_float(t[0]) + __uint_as_float(t[1]);
}
__device__ __forceinline__ float xrow_max(float x) {
  auto s = __builtin_amdgcn_permlane16_swap(__float_as_uint(x), __float_as_uint(x), false, false);
  x = fmaxf(__uint_as_float(s[0]), __uint_as_float(s[1]));
  auto t = __builtin_amdgcn_permlane32_swap(__float_as_uint(x), __float_as_uint(x), false, false);
  return fmaxf(__uint_as_float(t[0]), __uint_as_float(t[1]));
}
__device__ __forceinline__ float max4(f32x4 a) { return fmaxf(fmaxf(a[0], a[1]), fmaxf(a[2], a[3])); }
__device__ __forceinline__ float sum4(f32x4 a) { return (a[0] + a[1]) + (a[2] + a[3]); }
template <int W> __device__ __forceinline__ void waitvm() { asm volatile("s_waitcnt vmcnt(%0)" ::"n"(W) : "memory"); }
__device__ __forceinline__ void bar() { asm volatile("s_barrier" ::: "memory"); }
__device__ __forceinline__ void lds_bar() { asm volatile("s_waitcnt lgkmcnt(0)\n\ts_barrier" ::: "memory"); }
__device__ __forceinline__ void bars(int n) {
#pragma unroll 1
  for (; n > 0; --n) bar();
}
__device__ __forceinline__ void st128(gu128* p, u32x4 x) {
  asm volatile("global_store_dwordx4 %0, %1, off sc1\n\ts_nop 1" ::"v"(p), "v"(x) : "memory");
}

#define TILE                                                              \
  "buffer_load_dwordx4 %[vo], s[60:63], s64 offen nt lds\n\t"             \
  "buffer_load_dwordx4 %[vo], s[60:63], s64 offen offset:1024 nt lds\n\t" \
  "buffer_load_dwordx4 %[vo], s[60:63], s64 offen offset:2048 nt lds\n\t" \
  "buffer_load_dwordx4 %[vo], s[60:63], s64 offen offset:3072 nt lds\n\t" \
  "s_add_u32 s64, s64, s72\n\ts_add_u32 m0, m0, s72\n\ts_cmp_lg_u32 s64, s65\n\t"
#define LEVEL(n) "2:\n\ts_waitcnt vmcnt(" #n ")\n\t" TILE "s_cbranch_scc1 2b\n\ts_branch 3f\n\t"
__device__ __forceinline__ void loader(int wave, u64 ka, u64 va, unsigned voff, unsigned ldsb, unsigned so0,
                                       unsigned sos, unsigned rc) {
  unsigned rn;
  asm volatile(
      "s_cmp_lg_u32 %[wv], 0\n\t"
      "s_cbranch_scc1 9f\n\t"
      "s_mov_b32 s60, %[kl]\n\ts_and_b32 s61, %[kh], 0xffff\n\ts_mov_b32 s62, %[nr]\n\ts_mov_b32 s63, 0x20000\n\t"
      "s_mov_b32 m0, %[lb]\n\ts_mov_b32 s64, %[so0]\n\ts_add_u32 s65, %[so0], %[pre]\n\ts_movk_i32 s72, 0x1000\n"
      "2:\n\t" TILE "s_cbranch_scc1 2b\n\t"
      "s_setprio 3\n\t"
      "s_add_u32 s65, %[so0], 0x8000\n\ts_movk_i32 s66, %[off0]\n\ts_mov_b32 s67, 0x18000\n\ts_mov_b32 s68, 0x18000\n\t"
      "s_movk_i32 s69, %[seg1]\n\ts_mov_b32 s70, 0\n\ts_mov_b32 s71, 1\n\ts_movk_i32 s73, 0x4000\n"
      "1:\n\t"
      "ds_read_b32 %[rn], %[rc]\n\t"
      "s_getpc_b64 vcc\n\ts_add_u32 vcc_lo, vcc_lo, s66\n\ts_addc_u32 vcc_hi, vcc_hi, 0\n\ts_setpc_b64 vcc\n\t"
      LEVEL(13) LEVEL(14) LEVEL(15) LEVEL(16) LEVEL(17) LEVEL(18) LEVEL(19) LEVEL(20) LEVEL(21) LEVEL(22) LEVEL(23)
      LEVEL(24) LEVEL(28)
      "3:\n\t"
      "s_add_u32 s64, s64, s67\n\ts_xor_b32 s67, s67, s68\n\t"
      "s_add_u32 s73, s73, 0x4000\n\ts_and_b32 s73, s73, 0xffff\n\ts_add_u32 m0, s73, %[lb]\n\t"
      "s_waitcnt lgkmcnt(0)\n\t"
      "v_med3_i32 %[rn], %[rn], 0, 11\n\tv_mad_u32_u24 %[rn], %[rn], 56, 12\n\t"
      "s_sub_u32 s69, s69, 1\n\ts_cmp_lg_u32 s69, 0\n\ts_cbranch_scc1 5f\n\t"
      "s_add_u32 s70, s70, 1\n\ts_movk_i32 s69, %[seg]\n\ts_add_u32 s64, s64, %[nxt]\n\t"
      "s_cmp_lt_u32 s70, 4\n\ts_cbranch_scc1 5f\n\t"
      "s_mov_b32 s64, %[sos]\n\ts_mov_b32 s68, 0\n\ts_movk_i32 s69, %[solo]\n\t"
      "s_cmp_eq_u32 s70, 4\n\ts_cbranch_scc1 5f\n\t"
      "s_mov_b32 s64, %[so0]\n\ts_mov_b32 s68, 0x18000\n\ts_movk_i32 s69, %[seg]\n\ts_mov_b32 s70, 0\n\t"
      "s_mov_b32 s60, %[vl]\n\ts_and_b32 s61, %[vh], 0xffff\n"
      "5:\n\t"
      "s_add_u32 s65, s64, 0x4000\n\t"
      "s_add_u32 s71, s71, 1\n\t"
      "s_cmp_lt_u32 s71, 3\n\ts_cbranch_scc1 1b\n\t"
      "s_barrier\n\t"
      "s_cmp_eq_u32 s71, 3\n\ts_cbranch_scc1 1b\n\t"
      "v_readfirstlane_b32 s66, %[rn]\n\t"
      "s_cmpk_lt_u32 s71, 255\n\ts_cbranch_scc1 1b\n\t"
      "s_movk_i32 s66, 684\n\t"
      "s_cmpk_eq_u32 s71, 255\n\ts_cbranch_scc1 1b\n\t"
      "s_waitcnt vmcnt(16)\n\ts_barrier\n\ts_waitcnt vmcnt(0)\n\ts_barrier\n\ts_barrier\n\ts_endpgm\n"
      "9:"
      : [rn] "=&v"(rn)
      : [wv] "s"(wave), [kl] "s"((unsigned)ka), [kh] "s"((unsigned)(ka >> 32)), [vl] "s"((unsigned)va),
        [vh] "s"((unsigned)(va >> 32)), [vo] "v"(voff), [lb] "s"(ldsb), [so0] "s"(so0), [sos] "s"(sos), [rc] "v"(rc),
        [nr] "n"(7 * HEADB), [pre] "n"(PRE * 4096), [off0] "n"(12 + 56 * I0), [seg1] "n"(SEG - 1), [seg] "n"(SEG),
        [solo] "n"(SOLO), [nxt] "n"(2 * HEADB - 4 * SEG * GB)
      : "vcc", "scc", "memory", "s60", "s61", "s62", "s63", "s64", "s65", "s66", "s67", "s68", "s69", "s70", "s71",
        "s72", "s73");
}

__global__ void __launch_bounds__(448, 2) __attribute__((aligned(4096)))
attn_decode(const uint16_t* __restrict__ q, const uint16_t* __restrict__ k, const uint16_t* __restrict__ v,
            uint16_t* __restrict__ out, u64* __restrict__ ws) {
  __shared__ __attribute__((aligned(16))) char lds_[LDSB];
  LAS char* lds = (LAS char*)lds_;
  volatile LAS int* rctl = (volatile LAS int*)(lds + RCB);
  const int bid = blockIdx.x;
  const int mem = ((bid >> 1) + (bid >> 4)) & 3;
  const int h0 = (bid & ~7) + ((bid ^ (bid >> 3)) & 1);
  const int bh = h0 + 2 * mem;
  const int tid = threadIdx.x, lane = tid & 63, wave = __builtin_amdgcn_readfirstlane(tid >> 6);

  const u64 t0 = __builtin_amdgcn_s_memrealtime();
  loader(wave, (u64)k + (size_t)h0 * HEADB, (u64)v + (size_t)h0 * HEADB, lane * 16, (unsigned)(size_t)lds,
         mem * 2 * GB, mem * 2 * HEADB + 4 * SEG * GB, (unsigned)(size_t)(lds + RCB));

  if (wave == 6) {
    gu32* tw = (gu32*)(ws + TWO + (bid >> 3) * 32);
    const int stg = (bid >> 3) & 15;
    bars(8 + stg);
    unsigned x0, wv;
    const unsigned one = 1, ack = 256;
    asm volatile("s_mov_b64 exec, 1\n\tglobal_atomic_add %0, %1, %2, off sc0\n\ts_mov_b64 exec, -1"
                 : "=&v"(x0) : "v"(tw), "v"(one) : "memory");
    bars(24);
    asm volatile("s_waitcnt vmcnt(0)" : "+v"(x0)::"memory");
    const unsigned xs = __builtin_amdgcn_readfirstlane(x0);
    unsigned tag = xs;
    bool acked = false, bad = false;
#pragma unroll 1
    for (int it = 0; tag - xs - 257u >= 8u; ++it) {
      asm volatile("global_load_dword %0, %1, off sc1" : "=v"(wv) : "v"(tw) : "memory");
      if (it < 2) bars(4);
      asm volatile("s_waitcnt vmcnt(0)" : "+v"(wv)::"memory");
      tag = __builtin_amdgcn_readfirstlane(wv);
      if (tag - xs == 8 && !acked) {
        acked = true;
        if (lane == 0) asm volatile("global_atomic_add %0, %1, off" ::"v"(tw), "v"(ack) : "memory");
      }
      if (it == 0) bars(tag - xs - 257u >= 8u ? 28 : 32);
      else if (it > 1) __builtin_amdgcn_s_sleep(8);
      if (it > SPIN_CAP) { bad = true; break; }
    }
    int nb = 68 + stg;
    const int L = lane & 31;
    const LAS f32x4* red = (const LAS f32x4*)(lds + REDB) + L;
    const LAS f32x4* mx = (const LAS f32x4*)(lds + MXB);
    const LAS f32x4* ls = (const LAS f32x4*)(lds + LSB);
    gu128* sl0 = (gu128*)(ws + SLO) + ((bid >> 3) * 2 + (bid & 1)) * 120;
    f32x4 oown = {0.f, 0.f, 0.f, 0.f};
    u32x4 a0 = {0, 0, 0, 0}, a1 = a0, a2 = a0, b0 = a0, b1 = a0, b2 = a0;
#define LOAD3(x0, x1, x2, p)                                                                               \
  asm volatile("global_load_dwordx4 %0, %3, off sc1\n\tglobal_load_dwordx4 %1, %3, off offset:640 sc1\n\t" \
               "global_load_dwordx4 %2, %3, off offset:1280 sc1"                                           \
               : "=&v"(x0), "=&v"(x1), "=&v"(x2) : "v"(p) : "memory")
#pragma unroll 1
    for (int u = 0; u < 4; ++u) {
      const int E = 128 + SEG * (u + 1) + 1;
      bars(E - nb);
      f32x4 o = (red[0] + red[32]) + (red[64] + red[96]);
      u32x4 fl = {__float_as_uint(C2 * max4(mx[u])), tag, __float_as_uint(sum4(ls[u])), tag};
      asm volatile("s_waitcnt lgkmcnt(0)" : "+v"(o), "+v"(fl)::"memory");
      gu128* sl = sl0 + u * (64 * 120);
      if (u != mem) {
        sl += ((mem - u - 1) & 3) * 40;
        if (lane < 32) st128(sl + lane, __builtin_bit_cast(u32x4, o));
        bars(GA);
        waitvm<0>();
        if (lane < 8 && !bad) st128(sl + 32 + lane, fl);
        nb = E + GA;
      } else {
        oown = o;
        bars(GA + GB2);
#pragma unroll 1
        for (int it = 0;; ++it) {
          LOAD3(b0, b1, b2, sl + 32);
          if (it == 0) bars(GC);
          asm volatile("s_waitcnt vmcnt(0)" : "+v"(b0), "+v"(b1), "+v"(b2)::"memory");
          const bool ok = b0[1] == tag && b0[3] == tag && b1[1] == tag && b1[3] == tag && b2[1] == tag && b2[3] == tag;
          if (__all(ok)) break;
          if (bad || it > SPIN_CAP) { bad = true; break; }
          __builtin_amdgcn_s_sleep(4);
        }
        LOAD3(a0, a1, a2, sl + L);
        bars(GD);
        asm volatile("s_waitcnt vmcnt(0)" : "+v"(a0), "+v"(a1), "+v"(a2)::"memory");
        nb = E + GA + GB2 + GC + GD;
      }
    }
#undef LOAD3
    float mo = C2 * fmaxf(max4(mx[mem]), max4(mx[4]));
    asm volatile("" : "+v"(mo));
    const float lo = sum4(ls[mem]) + sum4(ls[4]);
    const float m0 = __uint_as_float(b0[0]), m1 = __uint_as_float(b1[0]), m2 = __uint_as_float(b2[0]);
    const float M = fmaxf(fmaxf(mo, m0), fmaxf(m1, m2));
    const float wo = __builtin_amdgcn_exp2f(mo - M), w0 = __builtin_amdgcn_exp2f(m0 - M);
    const float w1 = __builtin_amdgcn_exp2f(m1 - M), w2 = __builtin_amdgcn_exp2f(m2 - M);
    float inv = 1.f / (fmaf(wo, lo, w0 * __uint_as_float(b0[2])) + fmaf(w1, __uint_as_float(b1[2]), w2 * __uint_as_float(b2[2])));
    if (bad) inv = __builtin_nanf("");
    f32x4 pre = (oown * wo + __builtin_bit_cast(f32x4, a0) * w0) +
                (__builtin_bit_cast(f32x4, a1) * w1 + __builtin_bit_cast(f32x4, a2) * w2);
    pre *= inv;
    float cs = wo * inv;
    gu64* op = (gu64*)(out + bh * D) + L;
    asm volatile("s_waitcnt lgkmcnt(0)" : "+v"(pre), "+v"(cs), "+v"(op)::"memory");
    bars(257 - nb);
    const f32x4 f = pre + ((red[0] + red[32]) + (red[64] + red[96])) * cs;
    if (lane < 32) *op = __builtin_bit_cast(u64, __builtin_convertvector(f, bf16x4));
  } else if (wave == 5) {
    rctl[0] = I0;
    gu64* cw = (gu64*)ws;
    u64 basev;
    asm volatile("global_load_dwordx2 %0, %1, off sc1" : "=v"(basev) : "v"(cw + (lane & (NCP - 1)) * CST) : "memory");
    unsigned tref = 0, tprev = 0, tnow = 0;
    float sch = 0.f, tau = 0.f, fprev = 0.f, lagf = 0.f, lagp = 0.f, stl = 0.f;
    bool on = false;
    float nst = 0.f; unsigned tc1 = 0, tc4 = 0, tc16 = 0;
    auto steps = [&](int n) {
#pragma unroll 1
      for (; n > 0; --n) {
        lds_bar();
        tnow = (unsigned)__builtin_amdgcn_s_memrealtime();
        sch += tau;
        if (on) {
          float lag = (float)(int)(tnow - tref) - sch;
          if (lag - lagp > STALL) { const float jump = lag - lagp; sch += jump; stl += jump; nst += jump; lag = lagp; }
          lagp = lag;
          lagf += (lag - lagf) * 0.25f;
          rctl[0] = (int)rintf(fminf(fmaxf(I0F + lagf * (1.f / KP), 0.f), 11.f));
        }
      }
    };
    steps(OFS - 2);
    asm volatile("s_waitcnt vmcnt(0)" : "+v"(basev)::"memory");
#pragma unroll 1
    for (int cp = 1; cp < NCP; ++cp) {
      const unsigned town = tnow;
      stl = 0.f;
      if (cp == 1) tc1 = town; if (cp == 4) tc4 = town; if (cp == 16) tc16 = town;
      const u64 addv = (1ull << 40) + town;
      if ((((bid >> 3) - (bid & 7) * 4 - cp * 5) & 31) == 0 && lane == 0)
        asm volatile("global_atomic_add_x2 %0, %1, off" ::"v"(cw + cp * CST), "v"(addv) : "memory");
      steps(RDL);
      const unsigned trd = tnow;
      u64 rdv;
      asm volatile("global_load_dwordx2 %0, %1, off sc1" : "=v"(rdv) : "v"(cw + cp * CST) : "memory");
      steps(WDL);
      asm volatile("s_waitcnt vmcnt(0)" : "+v"(rdv)::"memory");
      const unsigned blo = __builtin_amdgcn_readlane((unsigned)basev, cp);
      const unsigned bhi = __builtin_amdgcn_readlane((unsigned)(basev >> 32), cp);
      const u64 d = rdv - (((u64)bhi << 32) | blo);
      const unsigned cnt = min((unsigned)(d >> 40), (unsigned)NPUB);
      const int rel = (int)((unsigned)d - cnt * town) + (int)((NPUB - cnt) * (trd - town));
      const float frac = (float)rel * (1.f / NPUB);
      const float iv = (float)(int)(town - tprev) + (frac - fprev);
      const float cur = sch + (float)(int)(tref - town);
      tau = cp > 2 ? tau + 0.2f * fminf(fmaxf(iv * (1.f / CPI) - tau, -0.6f), 0.6f) : TAU0;
      float nw = frac + stl + tau * (RDL + WDL);
      if (on) nw = cur + fminf(fmaxf(nw - cur, -40.f), 40.f);
      else lagf = -frac;
      tref = town;
      sch = nw;
      lagp = (float)(int)(tnow - town) - nw;
      on = true;
      tprev = town; fprev = frac;
      steps(CPI - RDL - WDL);
    }
    steps(CPI - OFS);
    bars(3);
    { const u64 te = __builtin_amdgcn_s_memrealtime(); const int ex = __builtin_amdgcn_readfirstlane((int)(bid == 77 ? 10.f * nst : 0.f)); const u64 tend = t0 + 20000 + (ex > 0 ? ex : 0); while (__builtin_amdgcn_s_memrealtime() < tend) __builtin_amdgcn_s_sleep(2); }
  } else {
    const int w = wave - 1, c = lane & 15, r = lane >> 4;
    u32x4 qp = *reinterpret_cast<const u32x4*>(q + h0 * D + 8 * c);
    const unsigned qw = reinterpret_cast<const unsigned*>(q + (h0 + 2 * w) * D)[lane];
    LAS f32x4* sc = (LAS f32x4*)(lds + SCB + w * SCW);
    {
      constexpr int NU = 32 - SEG;
      const int i = lane >> 2;
      const int sl = i < 4 * NU ? (i / NU) * 32 + SEG + i % NU : 128 + SOLO + (i - 4 * NU);
      sc[sl * 4 + (lane & 3)] = f32x4{-INFINITY, -INFINITY, -INFINITY, -INFINITY};
    }
    const LAS u32x4* ring = (const LAS u32x4*)lds + w * 256 + lane;
    u32x4 x[4];
    int j = 0;
#pragma unroll 1
    for (int seg = 0; seg < 5; ++seg) {
      const int n = seg < 4 ? SEG : SOLO;
      LAS f32x4* sp = sc + seg * 128 + r;
      float m = -INFINITY;
#pragma unroll 1
      for (int i0 = 0; i0 < n; ++i0, ++j) {
        lds_bar();
#pragma unroll
        for (int i = 0; i < 4; ++i) x[i] = ring[(j & 3) * 1024 + i * 64];
        f32x4 s;
#pragma unroll
        for (int i = 0; i < 4; ++i) {
          float d = dot2(x[i][0], qp[0], 0.f);
          d = dot2(x[i][1], qp[1], d); d = dot2(x[i][2], qp[2], d); d = dot2(x[i][3], qp[3], d);
          s[i] = row16_sum(d);
          m = fmaxf(m, s[i]);
        }
        if (c == 0) sp[i0 * 4] = s;
        if (j == 0) ((LAS unsigned*)(lds + QB))[w * 64 + lane] = qw;
      }
      ((LAS float*)(lds + MXB))[seg * 4 + w] = xrow_max(m);
      qp = ((const LAS u32x4*)(lds + QB))[(seg < 3 ? seg + 1 : mem) * 16 + c];
    }
#pragma unroll 1
    for (int seg = 0; seg < 5; ++seg) {
      const int n = seg < 4 ? SEG : SOLO;
      const LAS f32x4* sp = sc + seg * 128 + r;
      f32x2 acc[4];
#pragma unroll
      for (int i = 0; i < 4; ++i) acc[i] = f32x2{0.f, 0.f};
#pragma unroll 1
      for (int i0 = 0; i0 < n; ++i0, ++j) {
        lds_bar();
#pragma unroll
        for (int i = 0; i < 4; ++i) x[i] = ring[(j & 3) * 1024 + i * 64];
        if (i0 == 0) {
          const bool own = seg == mem || seg == 4;
          const f32x4 ma = ((const LAS f32x4*)(lds + MXB))[own ? mem : seg];
          const f32x4 mb = ((const LAS f32x4*)(lds + MXB))[own ? 4 : seg];
          const float mc = -(C2 * fmaxf(max4(ma), max4(mb)));
          LAS f32x4* e = sc + seg * 128 + lane;
          float l = 0.f;
#pragma unroll 1
          for (int y = 0; y < (seg < 4 ? 2 : 1); ++y) {
            f32x4 t = e[y * 64];
#pragma unroll
            for (int i = 0; i < 4; ++i) t[i] = __builtin_amdgcn_exp2f(fmaf(t[i], C2, mc));
            l += sum4(t);
            e[y * 64] = t;
          }
          l = row16_sum(l); l = swap32_add(l, l); l = swap16_add(l, l);
          ((LAS float*)(lds + LSB))[seg * 4 + w] = l;
          asm volatile("" ::: "memory");
        }
        const f32x4 p = sp[i0 * 4];
#pragma unroll
        for (int i = 0; i < 4; ++i)
#pragma unroll
          for (int e = 0; e < 4; ++e) {
            const f32x2 vv = {__builtin_bit_cast(float, x[i][e] << 16), __builtin_bit_cast(float, x[i][e] & 0xffff0000u)};
            acc[e] = __builtin_elementwise_fma(f32x2{p[i], p[i]}, vv, acc[e]);
          }
      }
      const float u0 = swap32_add(acc[0][0], acc[2][0]), u1 = swap32_add(acc[0][1], acc[2][1]);
      const float u2 = swap32_add(acc[1][0], acc[3][0]), u3 = swap32_add(acc[1][1], acc[3][1]);
      ((LAS f32x2*)(lds + REDB))[w * 64 + 4 * c + r] = f32x2{swap16_add(u0, u2), swap16_add(u1, u3)};
    }
    lds_bar();
  }
}
}

extern "C" void kernel_launch(void* const* d_in, const int* in_sizes, int n_in, void* d_out, int out_size, void* d_ws,
                              size_t ws_size, hipStream_t stream) {
  if (n_in != 3 || in_sizes[0] != NHEAD * D || out_size != NHEAD * D || ws_size < WS_NEED) return;
  attn_decode<<<NHEAD, 448, 0, stream>>>((const uint16_t*)d_in[0], (const uint16_t*)d_in[1], (const uint16_t*)d_in[2],
                                         (uint16_t*)d_out, (u64*)d_ws);
}
```

```cpp
#include <hip/hip_runtime.h>
#include <stdint.h>

namespace {
typedef unsigned long long u64;
using u32x4 = __attribute__((ext_vector_type(4))) unsigned;
using f32x4 = __attribute__((ext_vector_type(4))) float;
using f32x2 = __attribute__((ext_vector_type(2))) float;
using bf16x2 = __attribute__((ext_vector_type(2))) __bf16;
using bf16x4 = __attribute__((ext_vector_type(4))) __bf16;
#define LAS __attribute__((address_space(3)))
typedef __attribute__((address_space(1))) unsigned gu32;
typedef __attribute__((address_space(1))) unsigned long long gu64;
typedef __attribute__((address_space(1))) u32x4 gu128;

constexpr int SK = 8192, D = 128, NHEAD = 256;
constexpr unsigned HEADB = SK * D * 2;
constexpr int NG = 256, GB = 16384, RINGB = 4 * GB;
constexpr int SOLO = 8, SEG = (128 - SOLO) / 4;
constexpr int PRE = 6;
constexpr int GA = 2, GB2 = 1, GC = 2, GD = 1;
constexpr int NCP = 32, CPI = NG / NCP, NPUB = 8, OFS = 8, RDL = 4, WDL = 3, I0 = 5;
constexpr float I0F = 4.5f, KP = 30.f, STALL = 150.f, TAU0 = 53.2f;
constexpr int SCW = 144 * 64;
constexpr int SCB = RINGB, REDB = SCB + 4 * SCW, QB = REDB + 4 * 512, MXB = QB + 1024, LSB = MXB + 80, RCB = LSB + 80,
              LDSB = RCB + 16;
constexpr int CST = 512 + 32, TWO = NCP * CST, SLO = TWO + 32 * 32;
constexpr size_t WS_NEED = (size_t)SLO * 8 + (size_t)384 * 640;
constexpr float C2 = 0.088388347648318440f * 1.4426950408889634f;
constexpr u64 SLOT_TAB = 0x32500143002100ull;
constexpr int SPIN_CAP = 1 << 21;
static_assert(SEG % 2 == 0 && 4 * SEG + SOLO == 128 && 4 * (32 - SEG) + 16 - SOLO == 16 && GA + GB2 + GC + GD + 1 < SOLO &&
              RDL + WDL <= CPI && LDSB <= 160 * 1024, "layout");

__device__ __forceinline__ float dot2(unsigned a, unsigned b, float acc) {
  return __builtin_amdgcn_fdot2_f32_bf16(__builtin_bit_cast(bf16x2, a), __builtin_bit_cast(bf16x2, b), acc, false);
}
template <int CTRL> __device__ __forceinline__ float dpp(float x) {
  return __builtin_bit_cast(float, __builtin_amdgcn_mov_dpp(__builtin_bit_cast(int, x), CTRL, 0xf, 0xf, true));
}
__device__ __forceinline__ float row16_sum(float x) {
  x += dpp<0xB1>(x); x += dpp<0x4E>(x); x += dpp<0x141>(x); x += dpp<0x128>(x); return x;
}
__device__ __forceinline__ float swap32_add(float a, float b) {
  auto t = __builtin_amdgcn_permlane32_swap(__float_as_uint(a), __float_as_uint(b), false, false);
  return __uint_as_float(t[0]) + __uint_as_float(t[1]);
}
__device__ __forceinline__ float swap16_add(float a, float b) {
  auto t = __builtin_amdgcn_permlane16_swap(__float_as_uint(a), __float_as_uint(b), false, false);
  return __uint_as_float(t[0]) + __uint_as_float(t[1]);
}
__device__ __forceinline__ float xrow_max(float x) {
  auto s = __builtin_amdgcn_permlane16_swap(__float_as_uint(x), __float_as_uint(x), false, false);
  x = fmaxf(__uint_as_float(s[0]), __uint_as_float(s[1]));
  auto t = __builtin_amdgcn_permlane32_swap(__float_as_uint(x), __float_as_uint(x), false, false);
  return fmaxf(__uint_as_float(t[0]), __uint_as_float(t[1]));
}
__device__ __forceinline__ float max4(f32x4 a) { return fmaxf(fmaxf(a[0], a[1]), fmaxf(a[2], a[3])); }
__device__ __forceinline__ float sum4(f32x4 a) { return (a[0] + a[1]) + (a[2] + a[3]); }
template <int W> __device__ __forceinline__ void waitvm() { asm volatile("s_waitcnt vmcnt(%0)" ::"n"(W) : "memory"); }
__device__ __forceinline__ void bar() { asm volatile("s_barrier" ::: "memory"); }
__device__ __forceinline__ void lds_bar() { asm volatile("s_waitcnt lgkmcnt(0)\n\ts_barrier" ::: "memory"); }
__device__ __forceinline__ void bars(int n) {
#pragma unroll 1
  for (; n > 0; --n) bar();
}
__device__ __forceinline__ void st128(gu128* p, u32x4 x) {
  asm volatile("global_store_dwordx4 %0, %1, off sc1\n\ts_nop 1" ::"v"(p), "v"(x) : "memory");
}

#define TILE                                                              \
  "buffer_load_dwordx4 %[vo], s[60:63], s64 offen nt lds\n\t"             \
  "buffer_load_dwordx4 %[vo], s[60:63], s64 offen offset:1024 nt lds\n\t" \
  "buffer_load_dwordx4 %[vo], s[60:63], s64 offen offset:2048 nt lds\n\t" \
  "buffer_load_dwordx4 %[vo], s[60:63], s64 offen offset:3072 nt lds\n\t" \
  "s_add_u32 s64, s64, s72\n\ts_add_u32 m0, m0, s72\n\ts_cmp_lg_u32 s64, s65\n\t"
#define LEVEL(n) "2:\n\ts_waitcnt vmcnt(" #n ")\n\t" TILE "s_cbranch_scc1 2b\n\ts_branch 3f\n\t"
__device__ __forceinline__ void loader(int wave, u64 ka, u64 va, unsigned voff, unsigned ldsb, unsigned so0,
                                       unsigned sos, unsigned rc) {
  unsigned rn;
  asm volatile(
      "s_cmp_lg_u32 %[wv], 0\n\t"
      "s_cbranch_scc1 9f\n\t"
      "s_mov_b32 s60, %[kl]\n\ts_and_b32 s61, %[kh], 0xffff\n\ts_mov_b32 s62, %[nr]\n\ts_mov_b32 s63, 0x20000\n\t"
      "s_mov_b32 m0, %[lb]\n\ts_mov_b32 s64, %[so0]\n\ts_add_u32 s65, %[so0], %[pre]\n\ts_movk_i32 s72, 0x1000\n"
      "2:\n\t" TILE "s_cbranch_scc1 2b\n\t"
      "s_setprio 3\n\t"
      "s_add_u32 s65, %[so0], 0x8000\n\ts_movk_i32 s66, %[off0]\n\ts_mov_b32 s67, 0x18000\n\ts_mov_b32 s68, 0x18000\n\t"
      "s_movk_i32 s69, %[seg1]\n\ts_mov_b32 s70, 0\n\ts_mov_b32 s71, 1\n\ts_movk_i32 s73, 0x4000\n"
      "1:\n\t"
      "ds_read_b32 %[rn], %[rc]\n\t"
      "s_getpc_b64 vcc\n\ts_add_u32 vcc_lo, vcc_lo, s66\n\ts_addc_u32 vcc_hi, vcc_hi, 0\n\ts_setpc_b64 vcc\n\t"
      LEVEL(13) LEVEL(14) LEVEL(15) LEVEL(16) LEVEL(17) LEVEL(18) LEVEL(19) LEVEL(20) LEVEL(21) LEVEL(22) LEVEL(23)
      LEVEL(24) LEVEL(28)
      "3:\n\t"
      "s_add_u32 s64, s64, s67\n\ts_xor_b32 s67, s67, s68\n\t"
      "s_add_u32 s73, s73, 0x4000\n\ts_and_b32 s73, s73, 0xffff\n\ts_add_u32 m0, s73, %[lb]\n\t"
      "s_waitcnt lgkmcnt(0)\n\t"
      "v_med3_i32 %[rn], %[rn], 0, 11\n\tv_mad_u32_u24 %[rn], %[rn], 56, 12\n\t"
      "s_sub_u32 s69, s69, 1\n\ts_cmp_lg_u32 s69, 0\n\ts_cbranch_scc1 5f\n\t"
      "s_add_u32 s70, s70, 1\n\ts_movk_i32 s69, %[seg]\n\ts_add_u32 s64, s64, %[nxt]\n\t"
      "s_cmp_lt_u32 s70, 4\n\ts_cbranch_scc1 5f\n\t"
      "s_mov_b32 s64, %[sos]\n\ts_mov_b32 s68, 0\n\ts_movk_i32 s69, %[solo]\n\t"
      "s_cmp_eq_u32 s70, 4\n\ts_cbranch_scc1 5f\n\t"
      "s_mov_b32 s64, %[so0]\n\ts_mov_b32 s68, 0x18000\n\ts_movk_i32 s69, %[seg]\n\ts_mov_b32 s70, 0\n\t"
      "s_mov_b32 s60, %[vl]\n\ts_and_b32 s61, %[vh], 0xffff\n"
      "5:\n\t"
      "s_add_u32 s65, s64, 0x4000\n\t"
      "s_add_u32 s71, s71, 1\n\t"
      "s_cmp_lt_u32 s71, 3\n\ts_cbranch_scc1 1b\n\t"
      "s_barrier\n\t"
      "s_cmp_eq_u32 s71, 3\n\ts_cbranch_scc1 1b\n\t"
      "v_readfirstlane_b32 s66, %[rn]\n\t"
      "s_cmpk_lt_u32 s71, 255\n\ts_cbranch_scc1 1b\n\t"
      "s_movk_i32 s66, 684\n\t"
      "s_cmpk_eq_u32 s71, 255\n\ts_cbranch_scc1 1b\n\t"
      "s_waitcnt vmcnt(16)\n\ts_barrier\n\ts_waitcnt vmcnt(0)\n\ts_barrier\n\ts_barrier\n\ts_endpgm\n"
      "9:"
      : [rn] "=&v"(rn)
      : [wv] "s"(wave), [kl] "s"((unsigned)ka), [kh] "s"((unsigned)(ka >> 32)), [vl] "s"((unsigned)va),
        [vh] "s"((unsigned)(va >> 32)), [vo] "v"(voff), [lb] "s"(ldsb), [so0] "s"(so0), [sos] "s"(sos), [rc] "v"(rc),
        [nr] "n"(7 * HEADB), [pre] "n"(PRE * 4096), [off0] "n"(12 + 56 * I0), [seg1] "n"(SEG - 1), [seg] "n"(SEG),
        [solo] "n"(SOLO), [nxt] "n"(2 * HEADB - 4 * SEG * GB)
      : "vcc", "scc", "memory", "s60", "s61", "s62", "s63", "s64", "s65", "s66", "s67", "s68", "s69", "s70", "s71",
        "s72", "s73");
}

__global__ void __launch_bounds__(448, 2) __attribute__((aligned(4096)))
attn_decode(const uint16_t* __restrict__ q, const uint16_t* __restrict__ k, const uint16_t* __restrict__ v,
            uint16_t* __restrict__ out, u64* __restrict__ ws) {
  __shared__ __attribute__((aligned(16))) char lds_[LDSB];
  LAS char* lds = (LAS char*)lds_;
  volatile LAS int* rctl = (volatile LAS int*)(lds + RCB);
  const int bid = blockIdx.x;
  const int mem = ((bid >> 1) + (bid >> 4)) & 3;
  const int h0 = (bid & ~7) + ((bid ^ (bid >> 3)) & 1);
  const int bh = h0 + 2 * mem;
  const int tid = threadIdx.x, lane = tid & 63, wave = __builtin_amdgcn_readfirstlane(tid >> 6);

  loader(wave, (u64)k + (size_t)h0 * HEADB, (u64)v + (size_t)h0 * HEADB, lane * 16, (unsigned)(size_t)lds,
         mem * 2 * GB, mem * 2 * HEADB + 4 * SEG * GB, (unsigned)(size_t)(lds + RCB));

  if (wave == 6) {
    gu32* tw = (gu32*)(ws + TWO + (bid >> 3) * 32);
    const int stg = (bid >> 3) & 15;
    bars(8 + stg);
    unsigned x0, wv;
    const unsigned one = 1, ack = 256;
    asm volatile("s_mov_b64 exec, 1\n\tglobal_atomic_add %0, %1, %2, off sc0\n\ts_mov_b64 exec, -1"
                 : "=&v"(x0) : "v"(tw), "v"(one) : "memory");
    bars(24);
    asm volatile("s_waitcnt vmcnt(0)" : "+v"(x0)::"memory");
    const unsigned xs = __builtin_amdgcn_readfirstlane(x0);
    unsigned tag = xs;
    bool acked = false, bad = false;
#pragma unroll 1
    for (int it = 0; tag - xs - 257u >= 8u; ++it) {
      asm volatile("global_load_dword %0, %1, off sc1" : "=v"(wv) : "v"(tw) : "memory");
      if (it < 2) bars(4);
      asm volatile("s_waitcnt vmcnt(0)" : "+v"(wv)::"memory");
      tag = __builtin_amdgcn_readfirstlane(wv);
      if (tag - xs == 8 && !acked) {
        acked = true;
        if (lane == 0) asm volatile("global_atomic_add %0, %1, off" ::"v"(tw), "v"(ack) : "memory");
      }
      if (it == 0) bars(tag - xs - 257u >= 8u ? 28 : 32);
      else if (it > 1) __builtin_amdgcn_s_sleep(8);
      if (it > SPIN_CAP) { bad = true; break; }
    }
    int nb = 68 + stg;
    const int L = lane & 31;
    const LAS f32x4* red = (const LAS f32x4*)(lds + REDB) + L;
    const LAS f32x4* mx = (const LAS f32x4*)(lds + MXB);
    const LAS f32x4* ls = (const LAS f32x4*)(lds + LSB);
    gu128* sl0 = (gu128*)(ws + SLO) + ((bid >> 3) * 2 + (bid & 1)) * 40;
    auto slot = [&](int u, int h) { return sl0 + (int)((SLOT_TAB >> (16 * u + 4 * h)) & 15) * (64 * 40); };
    f32x4 oown = {0.f, 0.f, 0.f, 0.f};
    u32x4 a0 = {0, 0, 0, 0}, a1 = a0, a2 = a0, b0 = a0, b1 = a0, b2 = a0;
#define LOAD3(x0, x1, x2, o)                                                                    \
  asm volatile("global_load_dwordx4 %0, %3, off sc1\n\tglobal_load_dwordx4 %1, %4, off sc1\n\t" \
               "global_load_dwordx4 %2, %5, off sc1"                                              \
               : "=&v"(x0), "=&v"(x1), "=&v"(x2) : "v"(p0 + (o)), "v"(p1 + (o)), "v"(p2 + (o)) : "memory")
#pragma unroll 1
    for (int u = 0; u < 4; ++u) {
      const int E = 128 + SEG * (u + 1) + 1;
      bars(E - nb);
      f32x4 o = (red[0] + red[32]) + (red[64] + red[96]);
      const unsigned tg = tag + u;
      u32x4 fl = {__float_as_uint(C2 * max4(mx[u])), tg, __float_as_uint(sum4(ls[u])), tg};
      asm volatile("s_waitcnt lgkmcnt(0)" : "+v"(o), "+v"(fl)::"memory");
      if (u != mem) {
        gu128* sl = slot(u, mem);
        if (lane < 32) st128(sl + lane, __builtin_bit_cast(u32x4, o));
        bars(GA);
        waitvm<0>();
        if (lane < 8 && !bad) st128(sl + 32 + lane, fl);
        nb = E + GA;
      } else {
        oown = o;
        gu128 *p0 = slot(u, (u + 1) & 3), *p1 = slot(u, (u + 2) & 3), *p2 = slot(u, (u + 3) & 3);
        bars(GA + GB2);
#pragma unroll 1
        for (int it = 0;; ++it) {
          LOAD3(b0, b1, b2, 32);
          if (it == 0) bars(GC);
          asm volatile("s_waitcnt vmcnt(0)" : "+v"(b0), "+v"(b1), "+v"(b2)::"memory");
          const bool ok = b0[1] == tg && b0[3] == tg && b1[1] == tg && b1[3] == tg && b2[1] == tg && b2[3] == tg;
          if (__all(ok)) break;
          if (bad || it > SPIN_CAP) { bad = true; break; }
          __builtin_amdgcn_s_sleep(4);
        }
        LOAD3(a0, a1, a2, L);
        bars(GD);
        asm volatile("s_waitcnt vmcnt(0)" : "+v"(a0), "+v"(a1), "+v"(a2)::"memory");
        nb = E + GA + GB2 + GC + GD;
      }
    }
#undef LOAD3
    float mo = C2 * fmaxf(max4(mx[mem]), max4(mx[4]));
    asm volatile("" : "+v"(mo));
    const float lo = sum4(ls[mem]) + sum4(ls[4]);
    const float m0 = __uint_as_float(b0[0]), m1 = __uint_as_float(b1[0]), m2 = __uint_as_float(b2[0]);
    const float M = fmaxf(fmaxf(mo, m0), fmaxf(m1, m2));
    const float wo = __builtin_amdgcn_exp2f(mo - M), w0 = __builtin_amdgcn_exp2f(m0 - M);
    const float w1 = __builtin_amdgcn_exp2f(m1 - M), w2 = __builtin_amdgcn_exp2f(m2 - M);
    float inv = 1.f / (fmaf(wo, lo, w0 * __uint_as_float(b0[2])) + fmaf(w1, __uint_as_float(b1[2]), w2 * __uint_as_float(b2[2])));
    if (bad) inv = __builtin_nanf("");
    f32x4 pre = (oown * wo + __builtin_bit_cast(f32x4, a0) * w0) +
                (__builtin_bit_cast(f32x4, a1) * w1 + __builtin_bit_cast(f32x4, a2) * w2);
    pre *= inv;
    float cs = wo * inv;
    gu64* op = (gu64*)(out + bh * D) + L;
    asm volatile("s_waitcnt lgkmcnt(0)" : "+v"(pre), "+v"(cs), "+v"(op)::"memory");
    bars(257 - nb);
    const f32x4 f = pre + ((red[0] + red[32]) + (red[64] + red[96])) * cs;
    if (lane < 32) *op = __builtin_bit_cast(u64, __builtin_convertvector(f, bf16x4));
  } else if (wave == 5) {
    rctl[0] = I0;
    gu64* cw = (gu64*)ws;
    u64 basev;
    asm volatile("global_load_dwordx2 %0, %1, off sc1" : "=v"(basev) : "v"(cw + (lane & (NCP - 1)) * CST) : "memory");
    unsigned tref = 0, tprev = 0, tnow = 0;
    float sch = 0.f, tau = 0.f, fprev = 0.f, lagf = 0.f, lagp = 0.f, stl = 0.f;
    bool on = false;
    auto steps = [&](int n) {
#pragma unroll 1
      for (; n > 0; --n) {
        lds_bar();
        tnow = (unsigned)__builtin_amdgcn_s_memrealtime();
        sch += tau;
        if (on) {
          float lag = (float)(int)(tnow - tref) - sch;
          if (lag - lagp > STALL) { const float jump = lag - lagp; sch += jump; stl += jump; lag = lagp; }
          lagp = lag;
          lagf += (lag - lagf) * 0.25f;
          rctl[0] = (int)rintf(fminf(fmaxf(I0F + lagf * (1.f / KP), 0.f), 11.f));
        }
      }
    };
    steps(OFS - 2);
    asm volatile("s_waitcnt vmcnt(0)" : "+v"(basev)::"memory");
#pragma unroll 1
    for (int cp = 1; cp < NCP; ++cp) {
      const unsigned town = tnow;
      stl = 0.f;
      const u64 addv = (1ull << 40) + town;
      if ((((bid >> 3) - (bid & 7) * 4 - cp * 5) & 31) == 0 && lane == 0)
        asm volatile("global_atomic_add_x2 %0, %1, off" ::"v"(cw + cp * CST), "v"(addv) : "memory");
      steps(RDL);
      const unsigned trd = tnow;
      u64 rdv;
      asm volatile("global_load_dwordx2 %0, %1, off sc1" : "=v"(rdv) : "v"(cw + cp * CST) : "memory");
      steps(WDL);
      asm volatile("s_waitcnt vmcnt(0)" : "+v"(rdv)::"memory");
      const unsigned blo = __builtin_amdgcn_readlane((unsigned)basev, cp);
      const unsigned bhi = __builtin_amdgcn_readlane((unsigned)(basev >> 32), cp);
      const u64 d = rdv - (((u64)bhi << 32) | blo);
      const unsigned cnt = min((unsigned)(d >> 40), (unsigned)NPUB);
      const int rel = (int)((unsigned)d - cnt * town) + (int)((NPUB - cnt) * (trd - town));
      const float frac = (float)rel * (1.f / NPUB);
      const float iv = (float)(int)(town - tprev) + (frac - fprev);
      const float cur = sch + (float)(int)(tref - town);
      tau = cp > 2 ? tau + 0.2f * fminf(fmaxf(iv * (1.f / CPI) - tau, -0.6f), 0.6f) : TAU0;
      float nw = frac + stl + tau * (RDL + WDL);
      if (on) nw = cur + fminf(fmaxf(nw - cur, -40.f), 40.f);
      else lagf = -frac;
      tref = town;
      sch = nw;
      lagp = (float)(int)(tnow - town) - nw;
      on = true;
      tprev = town; fprev = frac;
      steps(CPI - RDL - WDL);
    }
    steps(CPI - OFS);
    bars(3);
  } else {
    const int w = wave - 1, c = lane & 15, r = lane >> 4;
    u32x4 qp = *reinterpret_cast<const u32x4*>(q + h0 * D + 8 * c);
    const unsigned qw = reinterpret_cast<const unsigned*>(q + (h0 + 2 * w) * D)[lane];
    LAS f32x4* sc = (LAS f32x4*)(lds + SCB + w * SCW);
    {
      constexpr int NU = 32 - SEG;
      const int i = lane >> 2;
      const int sl = i < 4 * NU ? (i / NU) * 32 + SEG + i % NU : 128 + SOLO + (i - 4 * NU);
      sc[sl * 4 + (lane & 3)] = f32x4{-INFINITY, -INFINITY, -INFINITY, -INFINITY};
    }
    const LAS u32x4* ring = (const LAS u32x4*)lds + w * 256 + lane;
    u32x4 x[4];
    int j = 0;
#pragma unroll 1
    for (int seg = 0; seg < 5; ++seg) {
      const int n = seg < 4 ? SEG : SOLO;
      LAS f32x4* sp = sc + seg * 128 + r;
      float m = -INFINITY;
#pragma unroll 1
      for (int i0 = 0; i0 < n; ++i0, ++j) {
        lds_bar();
#pragma unroll
        for (int i = 0; i < 4; ++i) x[i] = ring[(j & 3) * 1024 + i * 64];
        f32x4 s;
#pragma unroll
        for (int i = 0; i < 4; ++i) {
          float d = dot2(x[i][0], qp[0], 0.f);
          d = dot2(x[i][1], qp[1], d); d = dot2(x[i][2], qp[2], d); d = dot2(x[i][3], qp[3], d);
          s[i] = row16_sum(d);
          m = fmaxf(m, s[i]);
        }
        if (c == 0) sp[i0 * 4] = s;
        if (j == 0) ((LAS unsigned*)(lds + QB))[w * 64 + lane] = qw;
      }
      ((LAS float*)(lds + MXB))[seg * 4 + w] = xrow_max(m);
      qp = ((const LAS u32x4*)(lds + QB))[(seg < 3 ? seg + 1 : mem) * 16 + c];
    }
#pragma unroll 1
    for (int seg = 0; seg < 5; ++seg) {
      const int n = seg < 4 ? SEG : SOLO;
      const LAS f32x4* sp = sc + seg * 128 + r;
      f32x2 acc[4];
#pragma unroll
      for (int i = 0; i < 4; ++i) acc[i] = f32x2{0.f, 0.f};
#pragma unroll 1
      for (int i0 = 0; i0 < n; ++i0, ++j) {
        lds_bar();
#pragma unroll
        for (int i = 0; i < 4; ++i) x[i] = ring[(j & 3) * 1024 + i * 64];
        if (i0 == 0) {
          const bool own = seg == mem || seg == 4;
          const f32x4 ma = ((const LAS f32x4*)(lds + MXB))[own ? mem : seg];
          const f32x4 mb = ((const LAS f32x4*)(lds + MXB))[own ? 4 : seg];
          const float mc = -(C2 * fmaxf(max4(ma), max4(mb)));
          LAS f32x4* e = sc + seg * 128 + lane;
          float l = 0.f;
#pragma unroll 1
          for (int y = 0; y < (seg < 4 ? 2 : 1); ++y) {
            f32x4 t = e[y * 64];
#pragma unroll
            for (int i = 0; i < 4; ++i) t[i] = __builtin_amdgcn_exp2f(fmaf(t[i], C2, mc));
            l += sum4(t);
            e[y * 64] = t;
          }
          l = row16_sum(l); l = swap32_add(l, l); l = swap16_add(l, l);
          ((LAS float*)(lds + LSB))[seg * 4 + w] = l;
          asm volatile("" ::: "memory");
        }
        const f32x4 p = sp[i0 * 4];
#pragma unroll
        for (int i = 0; i < 4; ++i)
#pragma unroll
          for (int e = 0; e < 4; ++e) {
            const f32x2 vv = {__builtin_bit_cast(float, x[i][e] << 16), __builtin_bit_cast(float, x[i][e] & 0xffff0000u)};
            acc[e] = __builtin_elementwise_fma(f32x2{p[i], p[i]}, vv, acc[e]);
          }
      }
      const float u0 = swap32_add(acc[0][0], acc[2][0]), u1 = swap32_add(acc[0][1], acc[2][1]);
      const float u2 = swap32_add(acc[1][0], acc[3][0]), u3 = swap32_add(acc[1][1], acc[3][1]);
      ((LAS f32x2*)(lds + REDB))[w * 64 + 4 * c + r] = f32x2{swap16_add(u0, u2), swap16_add(u1, u3)};
    }
    lds_bar();
  }
}
}

extern "C" void kernel_launch(void* const* d_in, const int* in_sizes, int n_in, void* d_out, int out_size, void* d_ws,
                              size_t ws_size, hipStream_t stream) {
  if (n_in != 3 || in_sizes[0] != NHEAD * D || out_size != NHEAD * D || ws_size < WS_NEED) return;
  attn_decode<<<NHEAD, 448, 0, stream>>>((const uint16_t*)d_in[0], (const uint16_t*)d_in[1], (const uint16_t*)d_in[2],
                                         (uint16_t*)d_out, (u64*)d_ws);
}
```

```cpp
#include <hip/hip_runtime.h>
#include <stdint.h>

namespace {
typedef unsigned long long u64;
using u32x4 = __attribute__((ext_vector_type(4))) unsigned;
using f32x4 = __attribute__((ext_vector_type(4))) float;
using f32x2 = __attribute__((ext_vector_type(2))) float;
using bf16x2 = __attribute__((ext_vector_type(2))) __bf16;
using bf16x4 = __attribute__((ext_vector_type(4))) __bf16;
#define LAS __attribute__((address_space(3)))
typedef __attribute__((address_space(1))) unsigned gu32;
typedef __attribute__((address_space(1))) unsigned long long gu64;
typedef __attribute__((address_space(1))) u32x4 gu128;

constexpr int SK = 8192, D = 128, NHEAD = 256;
constexpr unsigned HEADB = SK * D * 2;
constexpr int NG = 256, GB = 16384, RINGB = 4 * GB;
constexpr int SOLO = 8, SEG = (128 - SOLO) / 4;
constexpr int PRE = 6;
constexpr int GA = 2, GB2 = 1, GC = 2, GD = 2;
constexpr int NCP = 32, CPI = NG / NCP, NPUB = 8, RDL = 4, WDL = 3, I0 = 5;
constexpr float I0F = 4.5f, KP = 30.f, STALL = 150.f, TAU0 = 53.2f;
constexpr int SCW = 144 * 64;
constexpr int SCB = RINGB, REDB = SCB + 4 * SCW, QB = REDB + 4 * 512, MXB = QB + 1024, LSB = MXB + 80, RCB = LSB + 80,
              LDSB = RCB + 16;
constexpr int CST = 512 + 32, TWO = NCP * CST, SLO = TWO + 32 * 32;
constexpr size_t WS_NEED = (size_t)SLO * 8 + (size_t)384 * 640;
constexpr float C2 = 0.088388347648318440f * 1.4426950408889634f;
constexpr u64 SLOT_TAB = 0x32500143002100ull;
constexpr int SPIN_CAP = 1 << 21;
static_assert(SEG % 2 == 0 && 4 * SEG + SOLO == 128 && 4 * (32 - SEG) + 16 - SOLO == 16 && GA + GB2 + GC + 1 < SOLO &&
              RDL + WDL <= CPI && LDSB <= 160 * 1024, "layout");

__device__ __forceinline__ float dot2(unsigned a, unsigned b, float acc) {
  return __builtin_amdgcn_fdot2_f32_bf16(__builtin_bit_cast(bf16x2, a), __builtin_bit_cast(bf16x2, b), acc, false);
}
template <int CTRL> __device__ __forceinline__ float dpp(float x) {
  return __builtin_bit_cast(float, __builtin_amdgcn_mov_dpp(__builtin_bit_cast(int, x), CTRL, 0xf, 0xf, true));
}
__device__ __forceinline__ float row16_sum(float x) {
  x += dpp<0xB1>(x); x += dpp<0x4E>(x); x += dpp<0x141>(x); x += dpp<0x128>(x); return x;
}
__device__ __forceinline__ float swap32_add(float a, float b) {
  auto t = __builtin_amdgcn_permlane32_swap(__float_as_uint(a), __float_as_uint(b), false, false);
  return __uint_as_float(t[0]) + __uint_as_float(t[1]);
}
__device__ __forceinline__ float swap16_add(float a, float b) {
  auto t = __builtin_amdgcn_permlane16_swap(__float_as_uint(a), __float_as_uint(b), false, false);
  return __uint_as_float(t[0]) + __uint_as_float(t[1]);
}
__device__ __forceinline__ float xrow_max(float x) {
  auto s = __builtin_amdgcn_permlane16_swap(__float_as_uint(x), __float_as_uint(x), false, false);
  x = fmaxf(__uint_as_float(s[0]), __uint_as_float(s[1]));
  auto t = __builtin_amdgcn_permlane32_swap(__float_as_uint(x), __float_as_uint(x), false, false);
  return fmaxf(__uint_as_float(t[0]), __uint_as_float(t[1]));
}
__device__ __forceinline__ float max4(f32x4 a) { return fmaxf(fmaxf(a[0], a[1]), fmaxf(a[2], a[3])); }
__device__ __forceinline__ float sum4(f32x4 a) { return (a[0] + a[1]) + (a[2] + a[3]); }
template <int W> __device__ __forceinline__ void waitvm() { asm volatile("s_waitcnt vmcnt(%0)" ::"n"(W) : "memory"); }
__device__ __forceinline__ void bar() { asm volatile("s_barrier" ::: "memory"); }
__device__ __forceinline__ void lds_bar() { asm volatile("s_waitcnt lgkmcnt(0)\n\ts_barrier" ::: "memory"); }
__device__ __forceinline__ void bars(int n) {
#pragma unroll 1
  for (; n > 0; --n) bar();
}
__device__ __forceinline__ void st128(gu128* p, u32x4 x) {
  asm volatile("global_store_dwordx4 %0, %1, off sc1\n\ts_nop 1" ::"v"(p), "v"(x) : "memory");
}

#define TILE                                                              \
  "buffer_load_dwordx4 %[vo], s[60:63], s64 offen nt lds\n\t"             \
  "buffer_load_dwordx4 %[vo], s[60:63], s64 offen offset:1024 nt lds\n\t" \
  "buffer_load_dwordx4 %[vo], s[60:63], s64 offen offset:2048 nt lds\n\t" \
  "buffer_load_dwordx4 %[vo], s[60:63], s64 offen offset:3072 nt lds\n\t" \
  "s_add_u32 s64, s64, s72\n\ts_add_u32 m0, m0, s72\n\ts_cmp_lg_u32 s64, s65\n\t"
#define LEVEL(n) "2:\n\ts_waitcnt vmcnt(" #n ")\n\t" TILE "s_cbranch_scc1 2b\n\ts_branch 3f\n\t"
__device__ __forceinline__ void loader(int wave, u64 ka, u64 va, unsigned voff, unsigned ldsb, unsigned so0,
                                       unsigned sos, unsigned rc) {
  unsigned rn;
  asm volatile(
      "s_cmp_lg_u32 %[wv], 0\n\t"
      "s_cbranch_scc1 9f\n\t"
      "s_mov_b32 s60, %[kl]\n\ts_and_b32 s61, %[kh], 0xffff\n\ts_mov_b32 s62, %[nr]\n\ts_mov_b32 s63, 0x20000\n\t"
      "s_mov_b32 m0, %[lb]\n\ts_mov_b32 s64, %[so0]\n\ts_add_u32 s65, %[so0], %[pre]\n\ts_movk_i32 s72, 0x1000\n"
      "2:\n\t" TILE "s_cbranch_scc1 2b\n\t"
      "s_setprio 3\n\t"
      "s_add_u32 s65, %[so0], 0x8000\n\ts_movk_i32 s66, %[off0]\n\ts_mov_b32 s67, 0x18000\n\ts_mov_b32 s68, 0x18000\n\t"
      "s_movk_i32 s69, %[seg1]\n\ts_mov_b32 s70, 0\n\ts_mov_b32 s71, 1\n\ts_movk_i32 s73, 0x4000\n"
      "1:\n\t"
      "ds_read_b32 %[rn], %[rc]\n\t"
      "s_getpc_b64 vcc\n\ts_add_u32 vcc_lo, vcc_lo, s66\n\ts_addc_u32 vcc_hi, vcc_hi, 0\n\ts_setpc_b64 vcc\n\t"
      LEVEL(13) LEVEL(14) LEVEL(15) LEVEL(16) LEVEL(17) LEVEL(18) LEVEL(19) LEVEL(20) LEVEL(21) LEVEL(22) LEVEL(23)
      LEVEL(24) LEVEL(28)
      "3:\n\t"
      "s_add_u32 s64, s64, s67\n\ts_xor_b32 s67, s67, s68\n\t"
      "s_add_u32 s73, s73, 0x4000\n\ts_and_b32 s73, s73, 0xffff\n\ts_add_u32 m0, s73, %[lb]\n\t"
      "s_waitcnt lgkmcnt(0)\n\t"
      "v_med3_i32 %[rn], %[rn], 0, 11\n\tv_mad_u32_u24 %[rn], %[rn], 56, 12\n\t"
      "s_sub_u32 s69, s69, 1\n\ts_cmp_lg_u32 s69, 0\n\ts_cbranch_scc1 5f\n\t"
      "s_add_u32 s70, s70, 1\n\ts_movk_i32 s69, %[seg]\n\ts_add_u32 s64, s64, %[nxt]\n\t"
      "s_cmp_lt_u32 s70, 4\n\ts_cbranch_scc1 5f\n\t"
      "s_mov_b32 s64, %[sos]\n\ts_mov_b32 s68, 0\n\ts_movk_i32 s69, %[solo]\n\t"
      "s_cmp_eq_u32 s70, 4\n\ts_cbranch_scc1 5f\n\t"
      "s_mov_b32 s64, %[so0]\n\ts_mov_b32 s68, 0x18000\n\ts_movk_i32 s69, %[seg]\n\ts_mov_b32 s70, 0\n\t"
      "s_mov_b32 s60, %[vl]\n\ts_and_b32 s61, %[vh], 0xffff\n"
      "5:\n\t"
      "s_add_u32 s65, s64, 0x4000\n\t"
      "s_add_u32 s71, s71, 1\n\t"
      "s_cmp_lt_u32 s71, 3\n\ts_cbranch_scc1 1b\n\t"
      "s_barrier\n\t"
      "s_cmp_eq_u32 s71, 3\n\ts_cbranch_scc1 1b\n\t"
      "v_readfirstlane_b32 s66, %[rn]\n\t"
      "s_cmpk_lt_u32 s71, 255\n\ts_cbranch_scc1 1b\n\t"
      "s_movk_i32 s66, 684\n\t"
      "s_cmpk_eq_u32 s71, 255\n\ts_cbranch_scc1 1b\n\t"
      "s_waitcnt vmcnt(16)\n\ts_barrier\n\ts_waitcnt vmcnt(0)\n\ts_barrier\n\ts_barrier\n\ts_endpgm\n"
      "9:"
      : [rn] "=&v"(rn)
      : [wv] "s"(wave), [kl] "s"((unsigned)ka), [kh] "s"((unsigned)(ka >> 32)), [vl] "s"((unsigned)va),
        [vh] "s"((unsigned)(va >> 32)), [vo] "v"(voff), [lb] "s"(ldsb), [so0] "s"(so0), [sos] "s"(sos), [rc] "v"(rc),
        [nr] "n"(7 * HEADB), [pre] "n"(PRE * 4096), [off0] "n"(12 + 56 * I0), [seg1] "n"(SEG - 1), [seg] "n"(SEG),
        [solo] "n"(SOLO), [nxt] "n"(2 * HEADB - 4 * SEG * GB)
      : "vcc", "scc", "memory", "s60", "s61", "s62", "s63", "s64", "s65", "s66", "s67", "s68", "s69", "s70", "s71",
        "s72", "s73");
}

__global__ void __launch_bounds__(448, 2) __attribute__((aligned(4096)))
attn_decode(const uint16_t* __restrict__ q, const uint16_t* __restrict__ k, const uint16_t* __restrict__ v,
            uint16_t* __restrict__ out, u64* __restrict__ ws) {
  __shared__ __attribute__((aligned(16))) char lds_[LDSB];
  LAS char* lds = (LAS char*)lds_;
  volatile LAS int* rctl = (volatile LAS int*)(lds + RCB);
  const int bid = blockIdx.x;
  const int mem = ((bid >> 1) + (bid >> 4)) & 3;
  const int h0 = (bid & ~7) + ((bid ^ (bid >> 3)) & 1);
  const int bh = h0 + 2 * mem;
  const int tid = threadIdx.x, lane = tid & 63, wave = __builtin_amdgcn_readfirstlane(tid >> 6);

  loader(wave, (u64)k + (size_t)h0 * HEADB, (u64)v + (size_t)h0 * HEADB, lane * 16, (unsigned)(size_t)lds,
         mem * 2 * GB, mem * 2 * HEADB + 4 * SEG * GB, (unsigned)(size_t)(lds + RCB));

  if (wave == 6) {
    gu32* tw = (gu32*)(ws + TWO + (bid >> 3) * 32);
    const int stg = (bid >> 3) & 15;
    bars(8 + stg);
    unsigned x0, wv;
    const unsigned one = 1, ack = 256;
    asm volatile("s_mov_b64 exec, 1\n\tglobal_atomic_add %0, %1, %2, off sc0\n\ts_mov_b64 exec, -1"
                 : "=&v"(x0) : "v"(tw), "v"(one) : "memory");
    bars(24);
    asm volatile("s_waitcnt vmcnt(0)" : "+v"(x0)::"memory");
    const unsigned xs = __builtin_amdgcn_readfirstlane(x0);
    unsigned tag = xs;
    bool acked = false, bad = false;
#pragma unroll 1
    for (int it = 0; tag - xs - 257u >= 8u; ++it) {
      asm volatile("global_load_dword %0, %1, off sc1" : "=v"(wv) : "v"(tw) : "memory");
      if (it < 2) bars(4);
      asm volatile("s_waitcnt vmcnt(0)" : "+v"(wv)::"memory");
      tag = __builtin_amdgcn_readfirstlane(wv);
      if (tag - xs == 8 && !acked) {
        acked = true;
        if (lane == 0) asm volatile("global_atomic_add %0, %1, off" ::"v"(tw), "v"(ack) : "memory");
      }
      if (it == 0) bars(tag - xs - 257u >= 8u ? 28 : 32);
      else if (it > 1) __builtin_amdgcn_s_sleep(8);
      if (it > SPIN_CAP) { bad = true; break; }
    }
    int nb = 68 + stg;
    const int L = lane & 31;
    const LAS f32x4* red = (const LAS f32x4*)(lds + REDB) + L;
    const LAS f32x4* mx = (const LAS f32x4*)(lds + MXB);
    const LAS f32x4* ls = (const LAS f32x4*)(lds + LSB);
    gu128* sl0 = (gu128*)(ws + SLO) + ((bid >> 3) * 2 + (bid & 1)) * 40;
    auto slot = [&](int u, int h) { return sl0 + (int)((SLOT_TAB >> (16 * u + 4 * h)) & 15) * (64 * 40); };
    f32x4 oown = {0.f, 0.f, 0.f, 0.f};
    u32x4 a0 = {0, 0, 0, 0}, a1 = a0, a2 = a0, b0 = a0, b1 = a0, b2 = a0;
#define LOAD3(x0, x1, x2, o)                                                                    \
  asm volatile("global_load_dwordx4 %0, %3, off sc1\n\tglobal_load_dwordx4 %1, %4, off sc1\n\t" \
               "global_load_dwordx4 %2, %5, off sc1"                                              \
               : "=&v"(x0), "=&v"(x1), "=&v"(x2) : "v"(p0 + (o)), "v"(p1 + (o)), "v"(p2 + (o)) : "memory")
#pragma unroll 1
    for (int u = 0; u < 4; ++u) {
      const int E = 128 + SEG * (u + 1) + 1;
      bars(E - nb);
      f32x4 o = (red[0] + red[32]) + (red[64] + red[96]);
      const unsigned tg = tag + u;
      u32x4 fl = {__float_as_uint(C2 * max4(mx[u])), tg, __float_as_uint(sum4(ls[u])), tg};
      asm volatile("s_waitcnt lgkmcnt(0)" : "+v"(o), "+v"(fl)::"memory");
      if (u != mem) {
        gu128* sl = slot(u, mem);
        if (lane < 32) st128(sl + lane, __builtin_bit_cast(u32x4, o));
        bars(GA);
        waitvm<0>();
        if (lane < 8 && !bad) st128(sl + 32 + lane, fl);
        nb = E + GA;
      } else {
        oown = o;
        gu128 *p0 = slot(u, (u + 1) & 3), *p1 = slot(u, (u + 2) & 3), *p2 = slot(u, (u + 3) & 3);
        bars(GA + GB2);
#pragma unroll 1
        for (int it = 0;; ++it) {
          LOAD3(b0, b1, b2, 32);
          if (it == 0) bars(GC);
          asm volatile("s_waitcnt vmcnt(0)" : "+v"(b0), "+v"(b1), "+v"(b2)::"memory");
          const bool ok = b0[1] == tg && b0[3] == tg && b1[1] == tg && b1[3] == tg && b2[1] == tg && b2[3] == tg;
          if (__all(ok)) break;
          if (bad || it > SPIN_CAP) { bad = true; break; }
          __builtin_amdgcn_s_sleep(4);
        }
        nb = E + GA + GB2 + GC;
        if (u < 3) {
          LOAD3(a0, a1, a2, L);
          bars(GD);
          asm volatile("s_waitcnt vmcnt(0)" : "+v"(a0), "+v"(a1), "+v"(a2)::"memory");
          nb += GD;
        }
      }
    }
    if (mem == 3) {
      gu128 *p0 = slot(3, 0), *p1 = slot(3, 1), *p2 = slot(3, 2);
      asm volatile("global_load_dwordx4 %0, %3, off sc1\n\tglobal_load_dwordx4 %1, %4, off sc1\n\t"
                   "global_load_dwordx4 %2, %5, off sc1"
                   : "+&v"(a0), "+&v"(a1), "+&v"(a2) : "v"(p0 + L), "v"(p1 + L), "v"(p2 + L) : "memory");
    }
#undef LOAD3
    float mo = C2 * fmaxf(max4(mx[mem]), max4(mx[4]));
    asm volatile("" : "+v"(mo));
    const float lo = sum4(ls[mem]) + sum4(ls[4]);
    const float m0 = __uint_as_float(b0[0]), m1 = __uint_as_float(b1[0]), m2 = __uint_as_float(b2[0]);
    const float M = fmaxf(fmaxf(mo, m0), fmaxf(m1, m2));
    const float wo = __builtin_amdgcn_exp2f(mo - M), w0 = __builtin_amdgcn_exp2f(m0 - M);
    const float w1 = __builtin_amdgcn_exp2f(m1 - M), w2 = __builtin_amdgcn_exp2f(m2 - M);
    float inv = 1.f / (fmaf(wo, lo, w0 * __uint_as_float(b0[2])) + fmaf(w1, __uint_as_float(b1[2]), w2 * __uint_as_float(b2[2])));
    if (bad) inv = __builtin_nanf("");
    float cs = wo * inv, c0 = w0 * inv, c1 = w1 * inv, c2 = w2 * inv;
    f32x4 pre = oown * cs;
    gu64* op = (gu64*)(out + bh * D) + L;
    asm volatile("s_waitcnt lgkmcnt(0)" : "+v"(pre), "+v"(cs), "+v"(c0), "+v"(c1), "+v"(c2), "+v"(op)::"memory");
    bars(256 - nb);
    asm volatile("s_waitcnt vmcnt(0)" : "+v"(a0), "+v"(a1), "+v"(a2)::"memory");
    pre = (pre + __builtin_bit_cast(f32x4, a0) * c0) + (__builtin_bit_cast(f32x4, a1) * c1 + __builtin_bit_cast(f32x4, a2) * c2);
    asm volatile("s_barrier" : "+v"(pre)::"memory");
    const f32x4 f = pre + ((red[0] + red[32]) + (red[64] + red[96])) * cs;
    if (lane < 32) *op = __builtin_bit_cast(u64, __builtin_convertvector(f, bf16x4));
  } else if (wave == 5) {
    rctl[0] = I0;
    gu64* cw = (gu64*)ws;
    u64 basev;
    asm volatile("global_load_dwordx2 %0, %1, off sc1" : "=v"(basev) : "v"(cw + (lane & (NCP - 1)) * CST) : "memory");
    unsigned tref = 0, tprev = 0, tnow = 0;
    float sch = 0.f, tau = 0.f, fprev = 0.f, lagf = 0.f, lagp = 0.f, stl = 0.f;
    bool on = false;
    auto steps = [&](int n) {
#pragma unroll 1
      for (; n > 0; --n) {
        lds_bar();
        tnow = (unsigned)__builtin_amdgcn_s_memrealtime();
        sch += tau;
        if (on) {
          float lag = (float)(int)(tnow - tref) - sch;
          if (lag - lagp > STALL) { const float jump = lag - lagp; sch += jump; stl += jump; lag = lagp; }
          lagp = lag;
          lagf += (lag - lagf) * 0.25f;
          rctl[0] = (int)rintf(fminf(fmaxf(I0F + lagf * (1.f / KP), 0.f), 11.f));
        }
      }
    };
    steps(CPI - 2);
    asm volatile("s_waitcnt vmcnt(0)" : "+v"(basev)::"memory");
#pragma unroll 1
    for (int cp = 1; cp < NCP - 1; ++cp) {
      const unsigned town = tnow;
      stl = 0.f;
      const u64 addv = (1ull << 40) + town;
      if ((((bid >> 3) - (bid & 7) * 4 - cp * 5) & 31) == 0 && lane == 0)
        asm volatile("global_atomic_add_x2 %0, %1, off" ::"v"(cw + cp * CST), "v"(addv) : "memory");
      steps(RDL);
      const unsigned trd = tnow;
      u64 rdv;
      asm volatile("global_load_dwordx2 %0, %1, off sc1" : "=v"(rdv) : "v"(cw + cp * CST) : "memory");
      steps(WDL);
      asm volatile("s_waitcnt vmcnt(0)" : "+v"(rdv)::"memory");
      const unsigned blo = __builtin_amdgcn_readlane((unsigned)basev, cp);
      const unsigned bhi = __builtin_amdgcn_readlane((unsigned)(basev >> 32), cp);
      const u64 d = rdv - (((u64)bhi << 32) | blo);
      const unsigned cnt = min((unsigned)(d >> 40), (unsigned)NPUB);
      const int rel = (int)((unsigned)d - cnt * town) + (int)((NPUB - cnt) * (trd - town));
      const float frac = (float)rel * (1.f / NPUB);
      const float iv = (float)(int)(town - tprev) + (frac - fprev);
      const float cur = sch + (float)(int)(tref - town);
      tau = cp > 2 ? tau + 0.2f * fminf(fmaxf(iv * (1.f / CPI) - tau, -0.6f), 0.6f) : TAU0;
      float nw = frac + stl + tau * (RDL + WDL);
      if (on) nw = cur + fminf(fmaxf(nw - cur, -40.f), 40.f);
      else lagf = -frac;
      tref = town;
      sch = nw;
      lagp = (float)(int)(tnow - town) - nw;
      on = true;
      tprev = town; fprev = frac;
      steps(CPI - RDL - WDL);
    }
    steps(CPI);
    bars(3);
  } else {
    const int w = wave - 1, c = lane & 15, r = lane >> 4;
    u32x4 qp = *reinterpret_cast<const u32x4*>(q + h0 * D + 8 * c);
    const unsigned qw = reinterpret_cast<const unsigned*>(q + (h0 + 2 * w) * D)[lane];
    LAS f32x4* sc = (LAS f32x4*)(lds + SCB + w * SCW);
    {
      constexpr int NU = 32 - SEG;
      const int i = lane >> 2;
      const int sl = i < 4 * NU ? (i / NU) * 32 + SEG + i % NU : 128 + SOLO + (i - 4 * NU);
      sc[sl * 4 + (lane & 3)] = f32x4{-INFINITY, -INFINITY, -INFINITY, -INFINITY};
    }
    const LAS u32x4* ring = (const LAS u32x4*)lds + w * 256 + lane;
    u32x4 x[4];
    int j = 0;
#pragma unroll 1
    for (int seg = 0; seg < 5; ++seg) {
      const int n = seg < 4 ? SEG : SOLO;
      LAS f32x4* sp = sc + seg * 128 + r;
      float m = -INFINITY;
#pragma unroll 1
      for (int i0 = 0; i0 < n; ++i0, ++j) {
        lds_bar();
#pragma unroll
        for (int i = 0; i < 4; ++i) x[i] = ring[(j & 3) * 1024 + i * 64];
        f32x4 s;
#pragma unroll
        for (int i = 0; i < 4; ++i) {
          float d = dot2(x[i][0], qp[0], 0.f);
          d = dot2(x[i][1], qp[1], d); d = dot2(x[i][2], qp[2], d); d = dot2(x[i][3], qp[3], d);
          s[i] = row16_sum(d);
          m = fmaxf(m, s[i]);
        }
        if (c == 0) sp[i0 * 4] = s;
        if (j == 0) ((LAS unsigned*)(lds + QB))[w * 64 + lane] = qw;
      }
      ((LAS float*)(lds + MXB))[seg * 4 + w] = xrow_max(m);
      qp = ((const LAS u32x4*)(lds + QB))[(seg < 3 ? seg + 1 : mem) * 16 + c];
    }
#pragma unroll 1
    for (int seg = 0; seg < 5; ++seg) {
      const int n = seg < 4 ? SEG : SOLO;
      const LAS f32x4* sp = sc + seg * 128 + r;
      f32x2 acc[4];
#pragma unroll
      for (int i = 0; i < 4; ++i) acc[i] = f32x2{0.f, 0.f};
#pragma unroll 1
      for (int i0 = 0; i0 < n; ++i0, ++j) {
        lds_bar();
#pragma unroll
        for (int i = 0; i < 4; ++i) x[i] = ring[(j & 3) * 1024 + i * 64];
        if (i0 == 0) {
          const bool own = seg == mem || seg == 4;
          const f32x4 ma = ((const LAS f32x4*)(lds + MXB))[own ? mem : seg];
          const f32x4 mb = ((const LAS f32x4*)(lds + MXB))[own ? 4 : seg];
          const float mc = -(C2 * fmaxf(max4(ma), max4(mb)));
          LAS f32x4* e = sc + seg * 128 + lane;
          float l = 0.f;
#pragma unroll 1
          for (int y = 0; y < (seg < 4 ? 2 : 1); ++y) {
            f32x4 t = e[y * 64];
#pragma unroll
            for (int i = 0; i < 4; ++i) t[i] = __builtin_amdgcn_exp2f(fmaf(t[i], C2, mc));
            l += sum4(t);
            e[y * 64] = t;
          }
          l = row16_sum(l); l = swap32_add(l, l); l = swap16_add(l, l);
          ((LAS float*)(lds + LSB))[seg * 4 + w] = l;
          asm volatile("" ::: "memory");
        }
        const f32x4 p = sp[i0 * 4];
#pragma unroll
        for (int i = 0; i < 4; ++i)
#pragma unroll
          for (int e = 0; e < 4; ++e) {
            const f32x2 vv = {__builtin_bit_cast(float, x[i][e] << 16), __builtin_bit_cast(float, x[i][e] & 0xffff0000u)};
            acc[e] = __builtin_elementwise_fma(f32x2{p[i], p[i]}, vv, acc[e]);
          }
      }
      const float u0 = swap32_add(acc[0][0], acc[2][0]), u1 = swap32_add(acc[0][1], acc[2][1]);
      const float u2 = swap32_add(acc[1][0], acc[3][0]), u3 = swap32_add(acc[1][1], acc[3][1]);
      ((LAS f32x2*)(lds + REDB))[w * 64 + 4 * c + r] = f32x2{swap16_add(u0, u2), swap16_add(u1, u3)};
    }
    lds_bar();
  }
}
}

extern "C" void kernel_launch(void* const* d_in, const int* in_sizes, int n_in, void* d_out, int out_size, void* d_ws,
                              size_t ws_size, hipStream_t stream) {
  if (n_in != 3 || in_sizes[0] != NHEAD * D || out_size != NHEAD * D || ws_size < WS_NEED) return;
  attn_decode<<<NHEAD, 448, 0, stream>>>((const uint16_t*)d_in[0], (const uint16_t*)d_in[1], (const uint16_t*)d_in[2],
                                         (uint16_t*)d_out, (u64*)d_ws);
}
```
